```python
import torch

def _softmax(x, axis=-1):
    return torch.softmax(x, dim=axis)

def _gelu(x):
    return torch.nn.functional.gelu(x, approximate='tanh')

def _silu(x):
    return torch.nn.functional.silu(x)

def _sigmoid(x):
    return torch.sigmoid(x)

def _layernorm(x, axis=-1, eps=1e-05):
    m = x.mean(dim=axis, keepdim=True)
    v = x.var(dim=axis, keepdim=True, unbiased=False)
    return (x - m) / torch.sqrt(v + eps)

def _scatter_add(tgt, idx, src, axis=0):
    return tgt.clone().index_add_(axis, idx, src)

def _segment_sum(src, seg, n):
    out = torch.zeros((n,) + src.shape[1:], dtype=src.dtype)
    return out.index_add_(0, seg, src)

def _pad(x, pw):
    flat = tuple((v for pair in reversed(pw) for v in pair))
    return torch.nn.functional.pad(x, flat)
kernel_name = 'adaLN_cond__gqa_block_cached__glu_gated_bilinear__decode'

def setup_inputs():

    _w = lambda a: (lambda t: t.to(torch.bfloat16) if t.is_floating_point() else t)(torch.as_tensor(a))

    def _A_setup():
        B, Sq, D = (1, 1, 640)
        Hq, Hkv, Sk, Dh = (32, 32, 4096, 192)
        assert Hq % Hkv == 0, (Hq, Hkv)
        return {'x': _w((lambda *s: torch.randn(*s, dtype=torch.bfloat16) * 0.1)(B, Sq, D)), 'k_cache': _w((lambda *s: torch.randn(*s, dtype=torch.bfloat16) * 0.8)(B, Hkv, Sk, Dh)), 'v_cache': _w((lambda *s: torch.randn(*s, dtype=torch.bfloat16) * 3.2)(B, Hkv, Sk, Dh)), 'wq': _w((lambda *s: torch.randn(*s, dtype=torch.bfloat16) * 0.8)(D, Hq * Dh) * D ** (-0.5)), 'wo': _w((lambda *s: torch.randn(*s, dtype=torch.bfloat16) * 3.2)(Hq * Dh, D) * (Hq * Dh) ** (-0.5))}
    _A_in = _A_setup()

    def _B_setup():
        B, S, D, F = (1, 1, 640, 1536)
        return {'x': _w((lambda *s: torch.randn(*s, dtype=torch.bfloat16) * 0.1)(B, S, D)), 'w_gate': _w((lambda *s: torch.randn(*s, dtype=torch.bfloat16) * 3.2)(D, F) * D ** (-0.5)), 'w_up': _w((lambda *s: torch.randn(*s, dtype=torch.bfloat16) * 3.2)(D, F) * D ** (-0.5)), 'w_down': _w((lambda *s: torch.randn(*s, dtype=torch.bfloat16) * 3.2)(F, D) * F ** (-0.5))}
    _B_in = _B_setup()
    return {'A_x': _A_in['x'], 'A_k_cache': _A_in['k_cache'], 'A_v_cache': _A_in['v_cache'], 'A_wq': _A_in['wq'], 'A_wo': _A_in['wo'], 'B_x': _B_in['x'], 'B_w_gate': _B_in['w_gate'], 'B_w_up': _B_in['w_up'], 'B_w_down': _B_in['w_down'], 'c': _w((lambda *s: torch.randn(*s, dtype=torch.bfloat16) * 0.4)(1, 640) * 8.0), 'w_mod': _w((lambda *s: torch.randn(*s, dtype=torch.bfloat16) * 0.4)(640, 6 * 640) * 640 ** (-0.5) * 8.0)}

def reference(A_x, A_k_cache, A_v_cache, A_wq, A_wo, B_x, B_w_gate, B_w_up, B_w_down, c, w_mod):

    def _A_ref(x, k_cache, v_cache, wq, wo):
        B, Sq, D = x.shape
        Hq, Dh = (32, 192)
        Hkv = k_cache.shape[1]
        rep = Hq // Hkv
        q = (lambda x, *d: x.permute(*d))((x @ wq).reshape(B, Sq, Hq, Dh), 0, 2, 1, 3)
        k = (lambda x, n, axis: torch.repeat_interleave(x, n, dim=axis))(k_cache, rep, axis=1)
        v = (lambda x, n, axis: torch.repeat_interleave(x, n, dim=axis))(v_cache, rep, axis=1)
        scores = torch.einsum('bhqd,bhkd->bhqk', q, k) * Dh ** (-0.5)
        p = _softmax(scores, axis=-1)
        o = torch.einsum('bhqk,bhkd->bhqd', p, v)
        return (lambda x, *d: x.permute(*d))(o, 0, 2, 1, 3).reshape(B, Sq, Hq * Dh) @ wo

    def _B_ref(x, w_gate, w_up, w_down):
        g = torch.einsum('bsd,df->bsf', x, w_gate)
        u = torch.einsum('bsd,df->bsf', x, w_up)
        h = g * u
        return torch.einsum('bsf,fd->bsd', h, w_down)
    x = A_x
    mod = (_silu(c) @ w_mod).reshape(c.shape[0], 6, 1, -1)
    sa, ca, ga, sb, cb, gb = (mod[:, 0].clone(), mod[:, 1].clone(), mod[:, 2].clone(), mod[:, 3].clone(), mod[:, 4].clone(), mod[:, 5].clone())
    h = _layernorm(x, axis=-1) * (1 + ca) + sa
    x = x + ga * _A_ref(h, A_k_cache, A_v_cache, A_wq, A_wo)
    h = _layernorm(x, axis=-1) * (1 + cb) + sb
    x = x + gb * _B_ref(h, B_w_gate, B_w_up, B_w_down)
    return x
```

```cpp
#include <hip/hip_runtime.h>
#include <hip/hip_bf16.h>
#include <cstdint>

#define MIMIC 1

typedef unsigned short u16;
typedef unsigned v4u __attribute__((ext_vector_type(4)));
typedef __bf16 bf16x2 __attribute__((ext_vector_type(2)));

constexpr int D = 640, HQ = 32, SK = 4096, DH = 192, F = 1536;

__device__ __forceinline__ float bf2f(unsigned b) { return __uint_as_float(b << 16); }
__device__ __forceinline__ float rbf(float f) {
  return __bfloat162float(__float2bfloat16(f));
}
__device__ __forceinline__ float rbf_always(float f) { return __bfloat162float(__float2bfloat16(f)); }
__device__ __forceinline__ float lo(unsigned w) { return __uint_as_float(w << 16); }
__device__ __forceinline__ float hi(unsigned w) { return __uint_as_float(w & 0xffff0000u); }

__device__ __forceinline__ void gemv64(const float* xs, const u16* __restrict__ W, int ldw, int K, float* red  ) {
  const int tid = threadIdx.x, c8 = tid & 7, r = tid >> 3;
  float acc[8];
#pragma unroll
  for (int j = 0; j < 8; ++j) acc[j] = 0.f;
  for (int k = r; k < K; k += 32) {
    const v4u w = *(const v4u*)(W + (size_t)k * ldw + c8 * 8);
    const float xv = xs[k];
#pragma unroll
    for (int j = 0; j < 4; ++j) { acc[2 * j] = fmaf(xv, lo(w[j]), acc[2 * j]); acc[2 * j + 1] = fmaf(xv, hi(w[j]), acc[2 * j + 1]); }
  }
#pragma unroll
  for (int j = 0; j < 8; ++j) red[r * 64 + c8 * 8 + j] = acc[j];
  __syncthreads();
  float s = 0.f;
  if (tid < 64) { for (int i = 0; i < 32; ++i) s += red[i * 64 + tid]; }
  __syncthreads();
  if (tid < 64) red[tid] = s;
  __syncthreads();
}

__device__ __forceinline__ float block_sum256(float v, float* sh) {
  for (int o = 32; o > 0; o >>= 1) v += __shfl_xor(v, o);
  __syncthreads();
  if ((threadIdx.x & 63) == 0) sh[threadIdx.x >> 6] = v;
  __syncthreads();
  return sh[0] + sh[1] + sh[2] + sh[3];
}

__device__ __forceinline__ void adaln(const float* xv, const float* ss, const float* cc, float* hs, float* sh) {
  float s = 0.f;
  for (int i = threadIdx.x; i < D; i += 256) s += xv[i];
  const float mean = block_sum256(s, sh) * (1.f / D);
  float q = 0.f;
  for (int i = threadIdx.x; i < D; i += 256) { const float d = xv[i] - mean; q += d * d; }
  const float var = block_sum256(q, sh) * (1.f / D);
  const float m = rbf(mean), v = rbf(var);
  const float sd = rbf(sqrtf(rbf(v + 1e-5f)));
  for (int i = threadIdx.x; i < D; i += 256) {
    const float ln = rbf(rbf(xv[i] - m) / sd);
    hs[i] = rbf(rbf(ln * rbf(1.f + cc[i])) + ss[i]);
  }
  __syncthreads();
}

__global__ void __launch_bounds__(256) k_mod(const u16* __restrict__ c, const u16* __restrict__ wmod, float* mod) {
  __shared__ float xs[D]; __shared__ float red[32 * 64];
  for (int i = threadIdx.x; i < D; i += 256) { const float v = bf2f(c[i]); xs[i] = rbf(v / (1.f + __expf(-v))); }
  __syncthreads();
  gemv64(xs, wmod + blockIdx.x * 64, 6 * D, D, red);
  if (threadIdx.x < 64) mod[blockIdx.x * 64 + threadIdx.x] = rbf(red[threadIdx.x]);
}

__global__ void __launch_bounds__(256) k_q(const u16* __restrict__ x, const float* mod, const u16* __restrict__ wq, float* q) {
  __shared__ float xv[D], hs[D]; __shared__ float red[32 * 64]; __shared__ float sh[4];
  for (int i = threadIdx.x; i < D; i += 256) xv[i] = bf2f(x[i]);
  __syncthreads();
  adaln(xv, mod, mod + D, hs, sh);
  gemv64(hs, wq + blockIdx.x * 64, HQ * DH, D, red);
  if (threadIdx.x < 64) q[blockIdx.x * 64 + threadIdx.x] = rbf_always(red[threadIdx.x]);
}

__device__ __forceinline__ float dot2(unsigned k, unsigned q, float acc) {
  return __builtin_amdgcn_fdot2_f32_bf16(__builtin_bit_cast(bf16x2, k), __builtin_bit_cast(bf16x2, q), acc, false);
}

__global__ void __launch_bounds__(512) k_attn(const float* q, const u16* __restrict__ K, const u16* __restrict__ V, float* part) {
  __shared__ float sm[8], sl[8], so[8][DH];
  const int h = blockIdx.x >> 3, s = blockIdx.x & 7, tid = threadIdx.x, lane = tid & 63, w = tid >> 6, r = lane >> 3, c = lane & 7;
  unsigned qp[3][4];
#pragma unroll
  for (int j = 0; j < 3; ++j)
#pragma unroll
    for (int k = 0; k < 4; ++k) {
      const float a = q[h * DH + j * 64 + c * 8 + 2 * k], b = q[h * DH + j * 64 + c * 8 + 2 * k + 1];
      qp[j][k] = (__float_as_uint(a) >> 16) | (__float_as_uint(b) & 0xffff0000u);
    }
  const size_t base = ((size_t)h * SK + s * 512 + w * 64 + r) * DH + c * 8;
  float sc[8];
#pragma unroll
  for (int t = 0; t < 8; ++t) {
    float d = 0.f;
#pragma unroll
    for (int j = 0; j < 3; ++j) {
      const v4u kk = __builtin_nontemporal_load((const v4u*)(K + base + (size_t)t * 8 * DH + j * 64));
#pragma unroll
      for (int k = 0; k < 4; ++k) d = dot2(kk[k], qp[j][k], d);
    }
    d += __shfl_xor(d, 1); d += __shfl_xor(d, 2); d += __shfl_xor(d, 4);
    sc[t] = rbf(rbf(d) * 0.07216878364870322f);
  }
  float m = sc[0];
#pragma unroll
  for (int t = 1; t < 8; ++t) m = fmaxf(m, sc[t]);
  m = fmaxf(m, __shfl_xor(m, 8)); m = fmaxf(m, __shfl_xor(m, 16)); m = fmaxf(m, __shfl_xor(m, 32));
  float l = 0.f, acc[24];
#pragma unroll
  for (int i = 0; i < 24; ++i) acc[i] = 0.f;
#pragma unroll
  for (int t = 0; t < 8; ++t) {
    const float p = __expf(sc[t] - m);
    l += p;
#pragma unroll
    for (int j = 0; j < 3; ++j) {
      const v4u vv = __builtin_nontemporal_load((const v4u*)(V + base + (size_t)t * 8 * DH + j * 64));
#pragma unroll
      for (int k = 0; k < 4; ++k) { acc[j * 8 + 2 * k] = fmaf(p, lo(vv[k]), acc[j * 8 + 2 * k]); acc[j * 8 + 2 * k + 1] = fmaf(p, hi(vv[k]), acc[j * 8 + 2 * k + 1]); }
    }
  }
  l += __shfl_xor(l, 8); l += __shfl_xor(l, 16); l += __shfl_xor(l, 32);
#pragma unroll
  for (int i = 0; i < 24; ++i) { acc[i] += __shfl_xor(acc[i], 8); acc[i] += __shfl_xor(acc[i], 16); acc[i] += __shfl_xor(acc[i], 32); }
  if (r == 0) {
#pragma unroll
    for (int j = 0; j < 3; ++j)
#pragma unroll
      for (int k = 0; k < 8; ++k) so[w][j * 64 + c * 8 + k] = acc[j * 8 + k];
    if (c == 0) { sm[w] = m; sl[w] = l; }
  }
  __syncthreads();
  if (tid < DH) {
    float M = sm[0];
    for (int i = 1; i < 8; ++i) M = fmaxf(M, sm[i]);
    float L = 0.f, o = 0.f;
    for (int i = 0; i < 8; ++i) { const float e = __expf(sm[i] - M); L += e * sl[i]; o += e * so[i][tid]; }
    part[blockIdx.x * 256 + tid] = o;
    if (tid == 0) { part[blockIdx.x * 256 + 192] = M; part[blockIdx.x * 256 + 193] = L; }
  }
}

__global__ void __launch_bounds__(256) k_wo(const float* part, const u16* __restrict__ wo, float* wpart) {
  __shared__ float os[DH]; __shared__ float red[32 * 64];
  const int h = blockIdx.y, tid = threadIdx.x;
  if (tid < DH) {
    float M = -INFINITY;
    for (int i = 0; i < 8; ++i) M = fmaxf(M, part[(h * 8 + i) * 256 + 192]);
    float L = 0.f, o = 0.f;
    for (int i = 0; i < 8; ++i) { const float e = __expf(part[(h * 8 + i) * 256 + 192] - M); L += e * part[(h * 8 + i) * 256 + 193]; o += e * part[(h * 8 + i) * 256 + tid]; }
    os[tid] = rbf(o / L);
  }
  __syncthreads();
  gemv64(os, wo + (size_t)h * DH * D + blockIdx.x * 64, D, DH, red);
  if (tid < 64) wpart[h * D + blockIdx.x * 64 + tid] = red[tid];
}

__global__ void __launch_bounds__(256) k_ffn(const u16* __restrict__ x, const float* mod, const float* wpart, const u16* __restrict__ wg,
                                             const u16* __restrict__ wu, const u16* __restrict__ wd, float* x2out, float* dpart) {
  __shared__ float xv[D], hs[D], g[64], h2[64]; __shared__ float red[32 * 64]; __shared__ float sh[4];
  const int tid = threadIdx.x;
  for (int i = tid; i < D; i += 256) {
    float a = 0.f;
    for (int h = 0; h < HQ; ++h) a += wpart[h * D + i];
    xv[i] = rbf(bf2f(x[i]) + rbf(mod[2 * D + i] * rbf(a)));
    if (blockIdx.x == 0) x2out[i] = xv[i];
  }
  __syncthreads();
  adaln(xv, mod + 3 * D, mod + 4 * D, hs, sh);
  gemv64(hs, wg + blockIdx.x * 64, F, D, red);
  if (tid < 64) g[tid] = rbf(red[tid]);
  __syncthreads();
  gemv64(hs, wu + blockIdx.x * 64, F, D, red);
  if (tid < 64) h2[tid] = rbf(g[tid] * rbf(red[tid]));
  __syncthreads();
  for (int cg = 0; cg < 10; ++cg) {
    gemv64(h2, wd + (size_t)blockIdx.x * 64 * D + cg * 64, D, 64, red);
    if (tid < 64) dpart[blockIdx.x * D + cg * 64 + tid] = red[tid];
    __syncthreads();
  }
}

__global__ void __launch_bounds__(640) k_out(const float* x2, const float* mod, const float* dpart, __hip_bfloat16* out) {
  const int i = threadIdx.x;
  float a = 0.f;
  for (int b = 0; b < 24; ++b) a += dpart[b * D + i];
  out[i] = __float2bfloat16(x2[i] + rbf(mod[5 * D + i] * rbf(a)));
}

extern "C" void kernel_launch(void* const* d_in, const int* in_sizes, int n_in, void* d_out, int out_size, void* d_ws, size_t ws_size,
                              hipStream_t stream) {
  const u16* x = (const u16*)d_in[0];
  const u16* K = (const u16*)d_in[1];
  const u16* V = (const u16*)d_in[2];
  const u16* wq = (const u16*)d_in[3];
  const u16* wo = (const u16*)d_in[4];
  const u16* wg = (const u16*)d_in[6];
  const u16* wu = (const u16*)d_in[7];
  const u16* wd = (const u16*)d_in[8];
  const u16* c = (const u16*)d_in[9];
  const u16* wmod = (const u16*)d_in[10];
  float* ws = (float*)d_ws;
  float* mod = ws;
  float* q = ws + 4096;
  float* part = ws + 12288;
  float* wpart = part + 65536;
  float* x2 = wpart + 32768;
  float* dpart = x2 + 1024;
  k_mod<<<60, 256, 0, stream>>>(c, wmod, mod);
  k_q<<<96, 256, 0, stream>>>(x, mod, wq, q);
  k_attn<<<256, 512, 0, stream>>>(q, K, V, part);
  k_wo<<<dim3(10, 32), 256, 0, stream>>>(part, wo, wpart);
  k_ffn<<<24, 256, 0, stream>>>(x, mod, wpart, wg, wu, wd, x2, dpart);
  k_out<<<1, 640, 0, stream>>>(x2, mod, dpart, (__hip_bfloat16*)d_out);
}
```

```cpp
#include <hip/hip_runtime.h>
#include <hip/hip_bf16.h>
#include <cstdint>

typedef unsigned short u16;
typedef unsigned v4u __attribute__((ext_vector_type(4)));
typedef unsigned v2u __attribute__((ext_vector_type(2)));
typedef float v4f __attribute__((ext_vector_type(4)));
typedef float v2f __attribute__((ext_vector_type(2)));
typedef __bf16 bf16x2 __attribute__((ext_vector_type(2)));
typedef __amdgpu_buffer_rsrc_t rsrc_t;
#define LAS __attribute__((address_space(3)))
#define GAS __attribute__((address_space(1)))
#define DEV __device__ __forceinline__

constexpr int D = 640, SK = 4096, DH = 192, FF = 1536;
constexpr int AUX_NT = 2, AUX_SC1 = 16;
constexpr int WS_EP = 0, WS_MODG = 4096, WS_QG = 32768, WS_MLG = 65536, WS_OP = 131072, WS_WP = 393216, WS_DP = 524288, WS_BYTES = 1 << 20;
constexpr unsigned SENT = 0xFFFFFFFFu;
constexpr unsigned SPIN_CAP = 1u << 17;
constexpr float LOG2E = 1.4426950408889634f;
constexpr float QSCALE = 0.07216878364870322f;

#define STAMP(i) do { } while (0)

constexpr int LDS_BIG = 0;
constexpr int LDS_RED = 81920;
constexpr int LDS_VEC = 88064;
constexpr int LDS_VEC2 = 90624;
constexpr int LDS_HB = 93184;
constexpr int LDS_QS = 94464;
constexpr int LDS_STAMP = 97536;
constexpr int LDS_BYTES = 97792;

DEV float lo16(unsigned w) { return __uint_as_float(w << 16); }
DEV float hi16(unsigned w) { return __uint_as_float(w & 0xffff0000u); }
DEV float rbf(float f) { return (float)(__bf16)f; }
DEV unsigned pack2(float a, float b) { bf16x2 v = {(__bf16)a, (__bf16)b}; return __builtin_bit_cast(unsigned, v); }
DEV float dot2(unsigned a, unsigned b, float acc) { return __builtin_amdgcn_fdot2_f32_bf16(__builtin_bit_cast(bf16x2, a), __builtin_bit_cast(bf16x2, b), acc, false); }
template <int CTRL> DEV float dpp(float x) { return __builtin_bit_cast(float, __builtin_amdgcn_mov_dpp(__builtin_bit_cast(int, x), CTRL, 0xf, 0xf, true)); }
constexpr int XOR1 = 0xB1, XOR2 = 0x4E, XOR7 = 0x141, XOR8 = 0x128;
DEV float fold32(float a, float b) { auto s = __builtin_amdgcn_permlane32_swap(__float_as_uint(a), __float_as_uint(b), false, false); return __uint_as_float(s[0]) + __uint_as_float(s[1]); }
DEV float fold16(float a, float b) { auto s = __builtin_amdgcn_permlane16_swap(__float_as_uint(a), __float_as_uint(b), false, false); return __uint_as_float(s[0]) + __uint_as_float(s[1]); }
DEV float max32(float a) { auto s = __builtin_amdgcn_permlane32_swap(__float_as_uint(a), __float_as_uint(a), false, false); return fmaxf(__uint_as_float(s[0]), __uint_as_float(s[1])); }
DEV float max16(float a) { auto s = __builtin_amdgcn_permlane16_swap(__float_as_uint(a), __float_as_uint(a), false, false); return fmaxf(__uint_as_float(s[0]), __uint_as_float(s[1])); }
DEV float wave_sum(float x) { x += dpp<XOR1>(x); x += dpp<XOR2>(x); x += dpp<XOR7>(x); x += dpp<XOR8>(x); x = fold16(x, x); return fold32(x, x); }
DEV float wave_max(float x) { x = fmaxf(x, dpp<XOR1>(x)); x = fmaxf(x, dpp<XOR2>(x)); x = fmaxf(x, dpp<XOR7>(x)); x = fmaxf(x, dpp<XOR8>(x)); x = max16(x); return max32(x); }
DEV rsrc_t mkrs(const void* p, int bytes) { return __builtin_amdgcn_make_buffer_rsrc((void*)p, 0, bytes, 0x00020000); }
DEV v4u ldnt(rsrc_t rs, int voff, int soff) { return __builtin_amdgcn_raw_buffer_load_b128(rs, voff, soff, AUX_NT); }
#define MEMCLOB() asm volatile("" ::: "memory")
DEV v4u ldsc4(rsrc_t rs, int voff) { return __builtin_amdgcn_raw_buffer_load_b128(rs, voff, 0, AUX_SC1); }
DEV v2u ldsc2(rsrc_t rs, int voff) { return __builtin_amdgcn_raw_buffer_load_b64(rs, voff, 0, AUX_SC1); }
DEV unsigned ldsc1(rsrc_t rs, int voff) { return __builtin_amdgcn_raw_buffer_load_b32(rs, voff, 0, AUX_SC1); }
DEV void stsc4(v4u v, rsrc_t rs, int voff) { __builtin_amdgcn_raw_buffer_store_b128(v, rs, voff, 0, AUX_SC1); }
DEV void stsc2(v2u v, rsrc_t rs, int voff) { __builtin_amdgcn_raw_buffer_store_b64(v, rs, voff, 0, AUX_SC1); }
DEV void stsc1(unsigned v, rsrc_t rs, int voff) { __builtin_amdgcn_raw_buffer_store_b32(v, rs, voff, 0, AUX_SC1); }
DEV unsigned guard(float f) { const unsigned u = __float_as_uint(f); return u < 0xFFFFFFFEu ? u : 0xFFFFFFFEu; }
DEV bool ok4(v4u v) { return (v.x != SENT) & (v.y != SENT) & (v.z != SENT) & (v.w != SENT); }
DEV int moff(bool on, int off) { return on ? off : 0x40000000; }
#define PIN24(a) asm volatile("" : "+v"(a[0]), "+v"(a[1]), "+v"(a[2]), "+v"(a[3]), "+v"(a[4]), "+v"(a[5]), "+v"(a[6]), "+v"(a[7]), "+v"(a[8]), "+v"(a[9]), "+v"(a[10]), "+v"(a[11]), \
  "+v"(a[12]), "+v"(a[13]), "+v"(a[14]), "+v"(a[15]), "+v"(a[16]), "+v"(a[17]), "+v"(a[18]), "+v"(a[19]), "+v"(a[20]), "+v"(a[21]), "+v"(a[22]), "+v"(a[23]))
template <int N> DEV void waitvm() { asm volatile("s_waitcnt vmcnt(%0)" ::"i"(N) : "memory"); }
#define SB() __builtin_amdgcn_sched_barrier(0)
DEV void lds_barrier() { asm volatile("s_waitcnt lgkmcnt(0)" ::: "memory"); __builtin_amdgcn_s_barrier(); asm volatile("" ::: "memory"); }
DEV void lds_fence() { asm volatile("s_waitcnt lgkmcnt(0)" ::: "memory"); }

DEV float reduce8_over32(const float (&a)[8], int lane) {
  float y[4], z[2];
#pragma unroll
  for (int i = 0; i < 4; ++i) y[i] = fold16(a[i], a[i + 4]);
#pragma unroll
  for (int i = 0; i < 2; ++i) { const float t0 = y[i] + dpp<XOR8>(y[i]), t1 = y[i + 2] + dpp<XOR8>(y[i + 2]); z[i] = (lane & 8) ? t1 : t0; }
  const float t0 = z[0] + dpp<XOR7>(z[0]), t1 = z[1] + dpp<XOR7>(z[1]);
  float v = (lane & 4) ? t1 : t0;
  v += dpp<XOR2>(v); v += dpp<XOR1>(v);
  return v;
}
DEV void pairperm(v4u e, v4u o, unsigned (&p)[8]) {
#pragma unroll
  for (int k = 0; k < 4; ++k) { p[2 * k] = __builtin_amdgcn_perm(o[k], e[k], 0x05040100u); p[2 * k + 1] = __builtin_amdgcn_perm(o[k], e[k], 0x07060302u); }
  asm volatile("" : "+v"(p[0]), "+v"(p[1]), "+v"(p[2]), "+v"(p[3]), "+v"(p[4]), "+v"(p[5]), "+v"(p[6]), "+v"(p[7]));
}
DEV void fma8(float (&acc)[24], int base, float p, v4u v) {
#pragma unroll
  for (int k = 0; k < 4; ++k) { acc[base + 2 * k] = fmaf(p, lo16(v[k]), acc[base + 2 * k]); acc[base + 2 * k + 1] = fmaf(p, hi16(v[k]), acc[base + 2 * k + 1]); }
}
DEV void reduce24_store(const float (&acc)[24], float f, LAS unsigned char* L, int w, int lane) {
  float y[12], z[6];
#pragma unroll
  for (int i = 0; i < 12; ++i) y[i] = fold32(acc[i], acc[i + 12]);
#pragma unroll
  for (int i = 0; i < 6; ++i) z[i] = fold16(y[i], y[i + 6]);
  float o[3];
#pragma unroll
  for (int i = 0; i < 3; ++i) { const float t0 = z[i] + dpp<XOR8>(z[i]), t1 = z[i + 3] + dpp<XOR8>(z[i + 3]); o[i] = ((lane & 8) ? t1 : t0) * f; }
  LAS float* red = (LAS float*)(L + LDS_RED) + w * 192 + (lane & 7) * 24 + ((lane >> 5) & 1) * 12 + ((lane >> 4) & 1) * 6 + ((lane >> 3) & 1) * 3;
  red[0] = o[0]; red[1] = o[1]; red[2] = o[2];
}
DEV v4f sum8_red(LAS unsigned char* L, int lane) {
  const LAS v4f* red = (const LAS v4f*)(L + LDS_RED) + lane;
  v4f t[8];
#pragma unroll
  for (int i = 0; i < 8; ++i) t[i] = red[i * 48];
  return ((t[0] + t[1]) + (t[2] + t[3])) + ((t[4] + t[5]) + (t[6] + t[7]));
}
DEV int slot4_dim(int l) { const int c = l / 6, a4 = l - c * 6; return (a4 >> 1) * 64 + c * 8 + (a4 & 1) * 4; }

DEV void mod_group_partial(const v4u (&raw)[10], LAS unsigned char* L, int redoff, int w, int lane) {
  const LAS float* sc = (const LAS float*)(L + LDS_VEC) + 80 * w + (lane >> 3);
  float a[8];
#pragma unroll
  for (int k = 0; k < 8; ++k) a[k] = 0.f;
#pragma unroll
  for (int i = 0; i < 10; ++i) { const float p = sc[8 * i];
#pragma unroll
    for (int k = 0; k < 4; ++k) { a[2 * k] = fmaf(p, lo16(raw[i][k]), a[2 * k]); a[2 * k + 1] = fmaf(p, hi16(raw[i][k]), a[2 * k + 1]); } }
  float y[4], z[2];
#pragma unroll
  for (int i = 0; i < 4; ++i) y[i] = fold32(a[i], a[i + 4]);
#pragma unroll
  for (int i = 0; i < 2; ++i) z[i] = fold16(y[i], y[i + 2]);
  const float t0 = z[0] + dpp<XOR8>(z[0]), t1 = z[1] + dpp<XOR8>(z[1]);
  ((LAS float*)(L + redoff))[w * 64 + (lane & 7) * 8 + ((lane >> 5) & 1) * 4 + ((lane >> 4) & 1) * 2 + ((lane >> 3) & 1)] = (lane & 8) ? t1 : t0;
}
DEV void mod_group_publish(LAS unsigned char* L, int redoff, int l32, int g, unsigned tag, rsrc_t rw) {
  const LAS v2f* red = (const LAS v2f*)(L + redoff) + l32;
  v2f s = red[0];
#pragma unroll
  for (int i = 1; i < 8; ++i) s += red[i * 32];
  stsc2(v2u{pack2(s.x, s.y), tag}, rw, WS_MODG + (g * 32 + l32) * 8);
}

__global__ void __launch_bounds__(512, 2)
mega(const u16* __restrict__ x, const u16* __restrict__ Kc, const u16* __restrict__ Vc, const u16* __restrict__ wq, const u16* __restrict__ wo,
     const u16* __restrict__ wg, const u16* __restrict__ wu, const u16* __restrict__ wd, const u16* __restrict__ cvec, const u16* __restrict__ wmod,
     u16* out, unsigned char* ws) {
  extern __shared__ __attribute__((aligned(16))) unsigned char smem[];
  LAS unsigned char* L = (LAS unsigned char*)smem;
  const int b = blockIdx.x, tid = threadIdx.x, lane = tid & 63, w = __builtin_amdgcn_readfirstlane(tid >> 6);
  const int r = lane >> 3, c = lane & 7;
  const rsrc_t rw = mkrs(ws, WS_BYTES);
  if (b >= 32) {
    const int j_ = b - 32, ii = j_ >> 3, hq = ii / 7, s = ii - hq * 7, h = (j_ & 7) * 4 + hq;
    const int ncu = s < 5 ? 71 : (s == 5 ? 78 : 79), t0cu = s < 5 ? 71 * s : (s == 5 ? 355 : 433);
    const int base = ncu >> 3, rem = ncu & 7, ex = w - (8 - rem);
    const int nt = base + (ex >= 0 ? 1 : 0), tw0 = t0cu + base * w + (ex > 0 ? ex : 0);
    const size_t kvoff = ((size_t)h * SK + (size_t)tw0 * 8) * DH;
    const rsrc_t rk = mkrs(Kc + kvoff, nt * 3072), rv = mkrs(Vc + kvoff, nt * 3072);
    const rsrc_t rwo = mkrs(wo + (size_t)(h * DH + 24 * w) * D + s * 128, s < 5 ? 24 * D * 2 : 0);
    const int voff = r * 384 + c * 16;
    const unsigned tag = ldsc1(rw, WS_EP) + 1u;
    if (w == 0) {
      const v4u sv = {SENT, SENT, SENT, SENT};
      stsc4(sv, rw, moff(lane < 48, WS_OP + (h * 8 + s) * 1024 + lane * 16));
      stsc4(sv, rw, moff(s < 5 && lane < 32, WS_WP + (h * D + s * 128) * 4 + lane * 16));
    }
    SB();
    v4u kr[10][3], vr[10][3];
#pragma unroll
    for (int t = 0; t < 10; ++t) {
      if (t == 2 && w == 0) waitvm<0>();
      if (t >= 2) waitvm<3>();
#pragma unroll
      for (int j = 0; j < 3; ++j) kr[t][j] = ldnt(rk, voff + j * 128, t * 3072);
      SB();
    }
    v4u qg;
#pragma unroll
    for (int t = 0; t < 6; ++t) {
      waitvm<3>();
      if (t == 5) qg = ldsc4(rw, WS_QG + h * 1024 + (lane < 48 ? lane : 47) * 16);
#pragma unroll
      for (int j = 0; j < 3; ++j) vr[t][j] = ldnt(rv, voff + j * 128, t * 3072);
      SB();
    }
    if (w == 0) STAMP(1);
    if (!__all(qg.y == tag && qg.w == tag)) {
#pragma unroll 1
      for (unsigned sp = 0; sp < SPIN_CAP; ++sp) {
        MEMCLOB();
        qg = ldsc4(rw, WS_QG + h * 1024 + (lane < 48 ? lane : 47) * 16);
        if (__all(qg.y == tag && qg.w == tag)) break;
        __builtin_amdgcn_s_sleep(2);
      }
    }
    if (w == 0) STAMP(2);
    LAS unsigned* qs = (LAS unsigned*)(L + LDS_QS) + w * 96;
    *(LAS v2u*)(qs + 2 * (lane < 48 ? lane : 47)) = v2u{qg.x, qg.z};
    lds_fence();
    v4u qp[3];
#pragma unroll
    for (int j = 0; j < 3; ++j) qp[j] = *(const LAS v4u*)(qs + j * 32 + c * 4);
    float sc[10];
#pragma unroll
    for (int t = 0; t < 10; ++t) {
      float d0 = 0.f, d1 = 0.f;
#pragma unroll
      for (int j = 0; j < 3; ++j) { d0 = dot2(kr[t][j][0], qp[j][0], d0); d1 = dot2(kr[t][j][1], qp[j][1], d1); d0 = dot2(kr[t][j][2], qp[j][2], d0); d1 = dot2(kr[t][j][3], qp[j][3], d1); }
      float d = d0 + d1;
      d += dpp<XOR1>(d); d += dpp<XOR2>(d); d += dpp<XOR7>(d);
      d = rbf(rbf(d) * QSCALE);
      sc[t] = t < nt ? d : -INFINITY;
      if (t == 0) {
        SB();
#pragma unroll
        for (int j = 0; j < 3; ++j) vr[6][j] = ldnt(rv, voff + j * 128, 6 * 3072);
        SB();
      }
    }
    float mw = sc[0];
#pragma unroll
    for (int t = 1; t < 10; ++t) mw = fmaxf(mw, sc[t]);
    mw = fmaxf(mw, dpp<XOR8>(mw)); mw = max16(mw); mw = max32(mw);
    float lw = 0.f;
#pragma unroll
    for (int t = 0; t < 10; ++t) { sc[t] = __builtin_amdgcn_exp2f((sc[t] - mw) * LOG2E); lw += sc[t]; }
    lw += dpp<XOR8>(lw); lw = fold16(lw, lw); lw = fold32(lw, lw);
    stsc4(v4u{__float_as_uint(mw), tag, __float_as_uint(lw), tag}, rw, moff(lane == 0, WS_MLG + h * 1024 + (s * 8 + w) * 16));
    SB();
    if (w == 0) STAMP(3);
    float acc[24];
#pragma unroll
    for (int i = 0; i < 24; ++i) acc[i] = 0.f;
    v4u wr[6], ml;
#define PV(t) do { _Pragma("unroll") for (int j = 0; j < 3; ++j) fma8(acc, j * 8, sc[t], vr[t][j]); PIN24(acc); SB(); } while (0)
#pragma unroll
    for (int k = 0; k < 3; ++k) {
      waitvm<3>();
      if (k == 2) ml = ldsc4(rw, WS_MLG + h * 1024 + (lane < 56 ? lane : 55) * 16);
#pragma unroll
      for (int j = 0; j < 3; ++j) vr[7 + k][j] = ldnt(rv, voff + j * 128, (7 + k) * 3072);
      SB();
      PV(2 * k); PV(2 * k + 1);
    }
    PV(6); PV(7);
#pragma unroll
    for (int i = 0; i < 6; ++i) wr[i] = ldnt(rwo, (lane >> 4) * (D * 2) + (lane & 15) * 16, i * 4 * D * 2);
    SB();
    PV(8);
    if (!__all(ml.y == tag && ml.w == tag)) {
#pragma unroll 1
      for (unsigned sp = 0; sp < SPIN_CAP; ++sp) {
        MEMCLOB();
        ml = ldsc4(rw, WS_MLG + h * 1024 + (lane < 56 ? lane : 55) * 16);
        if (__all(ml.y == tag && ml.w == tag)) break;
        __builtin_amdgcn_s_sleep(1);
      }
    }
    const float mi = __uint_as_float(ml.x), M = wave_max(mi);
    const float Ls = wave_sum(lane < 56 ? __uint_as_float(ml.z) * __builtin_amdgcn_exp2f((mi - M) * LOG2E) : 0.f);
    float fw = __builtin_amdgcn_exp2f((mw - M) * LOG2E) / Ls;
    asm volatile("" : "+v"(fw));
    SB();
    PV(9);
    if (w == 0) STAMP(4);
    reduce24_store(acc, fw, L, w, lane);
    lds_barrier();
    if (w == 0) {
      { const v4f o = sum8_red(L, lane < 48 ? lane : 47); stsc4(v4u{guard(o.x), guard(o.y), guard(o.z), guard(o.w)}, rw, moff(lane < 48, WS_OP + (h * 8 + s) * 1024 + lane * 16)); }
      STAMP(5);
    }
    if (s < 5) {
#pragma unroll
      for (int i = 0; i < 6; ++i) { const int row = 24 * w + 4 * i + (lane >> 4); *(LAS v4u*)(L + LDS_BIG + row * 256 + (((lane & 15) ^ ((row / 6) & 15)) << 4)) = wr[i]; }
      lds_barrier();
      const int rp = lane & 31, hf = lane >> 5;
      unsigned wp[3][8];
#pragma unroll
      for (int i = 0; i < 3; ++i) {
        const LAS unsigned char* p = L + LDS_BIG + (6 * rp + 2 * i) * 256 + (((2 * w + hf) ^ (rp & 15)) << 4);
        pairperm(*(const LAS v4u*)p, *(const LAS v4u*)(p + 256), wp[i]);
      }
      if (w == 1) {
        STAMP(6);
        const int l48 = lane < 48 ? lane : 47;
        v4u op[7];
#pragma unroll 1
        for (unsigned sp = 0; sp < SPIN_CAP; ++sp) {
        MEMCLOB();
          bool ok = true;
#pragma unroll
          for (int i = 0; i < 7; ++i) op[i] = ldsc4(rw, WS_OP + (h * 8 + i) * 1024 + l48 * 16);
#pragma unroll
          for (int i = 0; i < 7; ++i) ok &= ok4(op[i]);
          if (__all(ok)) break;
        }
        v4f o = __builtin_bit_cast(v4f, op[0]);
#pragma unroll
        for (int i = 1; i < 7; ++i) o += __builtin_bit_cast(v4f, op[i]);
        const int p0 = slot4_dim(l48) >> 1, p1 = p0 + 1;
        LAS unsigned* hb = (LAS unsigned*)(L + LDS_HB);
        hb[(p0 / 3) * 4 + p0 % 3] = pack2(o.x, o.y);
        hb[(p1 / 3) * 4 + p1 % 3] = pack2(o.z, o.w);
        STAMP(7);
      }
      lds_barrier();
      const v4u ov = *(const LAS v4u*)(L + LDS_HB + rp * 16);
      float a[8];
#pragma unroll
      for (int k = 0; k < 8; ++k) a[k] = dot2(wp[2][k], ov[2], dot2(wp[1][k], ov[1], dot2(wp[0][k], ov[0], 0.f)));
      const float v = reduce8_over32(a, lane);
      stsc1(guard(v), rw, moff((lane & 3) == 0, WS_WP + (h * D + s * 128 + 16 * w + (lane >> 2)) * 4));
      if (w == 0) STAMP(8);
    }
  } else {
    const int cj = b, hc = (b & 7) * 4 + (b >> 3);
    const bool ffn = cj < 24;
    const unsigned tag = ldsc1(rw, WS_EP) + 1u;
    if (w == 0 && ffn) {
      const v4u sv = {SENT, SENT, SENT, SENT};
      stsc4(sv, rw, WS_DP + cj * D * 4 + lane * 16); stsc4(sv, rw, WS_DP + cj * D * 4 + 1024 + lane * 16);
      if (lane < 32) stsc4(sv, rw, WS_DP + cj * D * 4 + 2048 + lane * 16);
    }
    const int t320 = tid < 320 ? tid : 319;
    const unsigned cpair = ((const GAS unsigned*)cvec)[t320], xpair = ((const GAS unsigned*)x)[t320];
    unsigned xall[5];
#pragma unroll
    for (int i = 0; i < 5; ++i) xall[i] = ((const GAS unsigned*)x)[lane + 64 * i];
    const rsrc_t rm = mkrs(wmod + (size_t)80 * w * (6 * D), 80 * 6 * D * 2);
    const rsrc_t rq = mkrs(wq + (size_t)80 * w * (32 * DH) + hc * DH, 80 * 32 * DH * 2 - hc * DH * 2);
    v4u mr[10], qr[10][3];
#pragma unroll
    for (int i = 0; i < 10; ++i) mr[i] = ldnt(rm, r * (6 * D * 2) + cj * 128 + c * 16, i * 8 * 6 * D * 2);
    SB();
#pragma unroll
    for (int i = 0; i < 10; ++i)
#pragma unroll
      for (int j = 0; j < 3; ++j) qr[i][j] = ldnt(rq, r * (32 * DH * 2) + j * 128 + c * 16, i * 8 * 32 * DH * 2);
    SB();
    {
      const float c0 = lo16(cpair), c1 = hi16(cpair);
      *(LAS v2f*)(L + LDS_VEC + t320 * 8) = v2f{rbf(c0 / (1.f + __expf(-c0))), rbf(c1 / (1.f + __expf(-c1)))};
    }
    float ln0, ln1;
    {
      float sm = 0.f;
#pragma unroll
      for (int i = 0; i < 5; ++i) sm += lo16(xall[i]) + hi16(xall[i]);
      const float mean = wave_sum(sm) * (1.f / D);
      float sq = 0.f;
#pragma unroll
      for (int i = 0; i < 5; ++i) { const float a = lo16(xall[i]) - mean, bb = hi16(xall[i]) - mean; sq += a * a + bb * bb; }
      const float var = wave_sum(sq) * (1.f / D);
      const float mb = rbf(mean), sd = rbf(sqrtf(rbf(rbf(var) + 1e-5f)));
      ln0 = rbf(rbf(lo16(xpair) - mb) / sd); ln1 = rbf(rbf(hi16(xpair) - mb) / sd);
    }
    asm volatile("" ::"v"(tag));
    lds_barrier();
    mod_group_partial(mr, L, LDS_RED, w, lane);
    lds_barrier();
    if (w == 0) { if (lane < 32) mod_group_publish(L, LDS_RED, lane, cj, tag, rw); STAMP(1); }
    if (w >= 3) {
      const int t = tid - 192;
      v2u ga, gc;
#pragma unroll 1
      for (unsigned sp = 0; sp < SPIN_CAP; ++sp) {
        MEMCLOB();
        ga = ldsc2(rw, WS_MODG + t * 8); gc = ldsc2(rw, WS_MODG + (320 + t) * 8);
        if (__all(ga.y == tag && gc.y == tag)) break;
        __builtin_amdgcn_s_sleep(1);
      }
      *(LAS v2u*)(L + LDS_VEC2 + t * 8) = v2u{ga.x, gc.x};
    }
    lds_barrier();
    LAS float* hs = (LAS float*)(L + LDS_BIG);
    if (tid < 320) {
      const v2u g = *(const LAS v2u*)(L + LDS_VEC2 + tid * 8);
      *(LAS v2f*)(hs + 2 * tid) = v2f{rbf(rbf(ln0 * rbf(1.f + lo16(g.y))) + lo16(g.x)), rbf(rbf(ln1 * rbf(1.f + hi16(g.y))) + hi16(g.x))};
    }
    if (w == 0) STAMP(2);
    lds_barrier();
    {
      float acc[24];
#pragma unroll
      for (int i = 0; i < 24; ++i) acc[i] = 0.f;
#pragma unroll
      for (int i = 0; i < 10; ++i) { const float p = hs[80 * w + 8 * i + r];
#pragma unroll
        for (int j = 0; j < 3; ++j) fma8(acc, j * 8, p, qr[i][j]); }
      reduce24_store(acc, 1.f, L, w, lane);
    }
    lds_barrier();
    if (w == 0) {
      waitvm<0>();
      if (lane < 48) { const v4f q = sum8_red(L, lane); stsc4(v4u{pack2(q.x, q.y), tag, pack2(q.z, q.w), tag}, rw, WS_QG + hc * 1024 + slot4_dim(lane) * 4); }
      STAMP(3);
    }
    SB();
    if (!ffn) {
      v4u m4[4][10];
#pragma unroll
      for (int gI = 0; gI < 4; ++gI) {
        const int g = 32 + (cj - 24) + 8 * gI;
        const rsrc_t rg = mkrs(wmod + (size_t)80 * w * (6 * D), g < 60 ? 80 * 6 * D * 2 : 0);
#pragma unroll
        for (int i = 0; i < 10; ++i) m4[gI][i] = ldnt(rg, r * (6 * D * 2) + g * 128 + c * 16, i * 8 * 6 * D * 2);
      }
      SB();
#pragma unroll
      for (int gI = 0; gI < 4; ++gI) mod_group_partial(m4[gI], L, LDS_BIG + 4096 + gI * 2048, w, lane);
      lds_barrier();
      if (w < 4) { const int g = 32 + (cj - 24) + 8 * w; if (g < 60 && lane < 32) mod_group_publish(L, LDS_BIG + 4096 + w * 2048, lane, g, tag, rw); }
      if (w == 0) STAMP(4);
    } else {
      const rsrc_t rg = mkrs(wg + cj * 64, D * FF * 2 - cj * 128), ru = mkrs(wu + cj * 64, D * FF * 2 - cj * 128);
      const rsrc_t rd = mkrs(wd + (size_t)cj * 64 * D + w * 128, w < 5 ? 64 * D * 2 - w * 256 : 0);
      v4u gr[2][5], ur[2][5], dr[8][2];
#pragma unroll
      for (int p = 0; p < 2; ++p)
#pragma unroll
        for (int i = 0; i < 5; ++i) {
          gr[p][i] = ldnt(rg, (40 * w + r) * (FF * 2) + c * 16, (320 * p + 8 * i) * FF * 2);
          ur[p][i] = ldnt(ru, (40 * w + r) * (FF * 2) + c * 16, (320 * p + 8 * i) * FF * 2);
        }
#pragma unroll
      for (int i = 0; i < 8; ++i)
#pragma unroll
        for (int e = 0; e < 2; ++e) dr[i][e] = ldnt(rd, (lane >> 4) * (16 * D * 2) + (lane & 15) * 16, (2 * i + e) * D * 2);
      SB();
      const int rp = lane & 31, hf = lane >> 5;
      unsigned gu[10][8], dw[8][8];
#pragma unroll
      for (int p = 0; p < 2; ++p) {
        lds_barrier();
#pragma unroll
        for (int i = 0; i < 5; ++i) {
          const int row = 40 * w + 8 * i + r, a = row * 128 + ((c ^ ((row / 10) & 7)) << 4);
          *(LAS v4u*)(L + LDS_BIG + a) = gr[p][i]; *(LAS v4u*)(L + LDS_BIG + 40960 + a) = ur[p][i];
        }
        lds_barrier();
#pragma unroll
        for (int i = 0; i < 5; ++i) {
          const LAS unsigned char* q = L + LDS_BIG + hf * 40960 + (10 * rp + 2 * i) * 128 + ((w ^ (rp & 7)) << 4);
          pairperm(*(const LAS v4u*)q, *(const LAS v4u*)(q + 128), gu[p * 5 + i]);
        }
      }
#pragma unroll
      for (int i = 0; i < 8; ++i) pairperm(dr[i][0], dr[i][1], dw[i]);
      if (w == 0) STAMP(4);
      float gbv = 0.f; unsigned gap = 0, sbp = 0, cbp = 0, xp = 0;
      const int ocol = 32 * cj + 4 * c + 2 * (lane >> 5) + ((lane >> 4) & 1);
      const int pi = 64 * w + 2 * rp + hf;
      if (w < 5) {
        xp = ((const GAS unsigned*)x)[pi];
#pragma unroll 1
        for (unsigned sp = 0; sp < SPIN_CAP; ++sp) {
        MEMCLOB();
          const v2u a = ldsc2(rw, WS_MODG + (640 + pi) * 8), bb = ldsc2(rw, WS_MODG + (960 + pi) * 8), cc = ldsc2(rw, WS_MODG + (1280 + pi) * 8);
          const v2u ak = ldsc2(rw, WS_MLG + rp * 1024 + w * 128);
          gap = a.x; sbp = bb.x; cbp = cc.x;
          if (__all(a.y == tag && bb.y == tag && cc.y == tag && ak.y == tag)) break;
          __builtin_amdgcn_s_sleep(8);
        }
      } else if (w == 5 && cj < 20) {
#pragma unroll 1
        for (unsigned sp = 0; sp < SPIN_CAP; ++sp) {
        MEMCLOB();
          const v2u a = ldsc2(rw, WS_MODG + (1600 + (ocol >> 1)) * 8);
          gbv = (ocol & 1) ? hi16(a.x) : lo16(a.x);
          if (__all(a.y == tag)) break;
          __builtin_amdgcn_s_sleep(8);
        }
      }
      if (w == 0) STAMP(5);
      float x20 = 0.f, x21 = 0.f;
      LAS v2f* stat = (LAS v2f*)(L + LDS_QS + 256);
      if (w < 5) {
        v4u pv[16];
#pragma unroll 1
        for (unsigned sp = 0; sp < SPIN_CAP; ++sp) {
        MEMCLOB();
          bool ok = true;
#pragma unroll
          for (int i = 0; i < 16; ++i) pv[i] = ldsc4(rw, WS_WP + ((2 * i + hf) * D + 128 * w + 4 * rp) * 4);
#pragma unroll
          for (int i = 0; i < 16; ++i) ok &= ok4(pv[i]);
          if (__all(ok)) break;
          __builtin_amdgcn_s_sleep(1);
        }
        if (w == 0) STAMP(6);
        v4f sm = __builtin_bit_cast(v4f, pv[0]);
#pragma unroll
        for (int i = 1; i < 16; ++i) sm += __builtin_bit_cast(v4f, pv[i]);
        const float a0 = rbf(fold32(sm.x, sm.z)), a1 = rbf(fold32(sm.y, sm.w));
        x20 = rbf(lo16(xp) + rbf(lo16(gap) * a0)); x21 = rbf(hi16(xp) + rbf(hi16(gap) * a1));
        *(LAS v2f*)(L + LDS_VEC2 + pi * 8) = v2f{x20, x21};
        const float piv = __builtin_bit_cast(float, __builtin_amdgcn_readfirstlane(__builtin_bit_cast(int, x20)));
        const float d0 = x20 - piv, d1 = x21 - piv;
        const float s1 = wave_sum(d0 + d1), s2 = wave_sum(d0 * d0 + d1 * d1);
        if (lane == 0) stat[w] = v2f{piv + s1 * (1.f / 128), s2 - s1 * s1 * (1.f / 128)};
      }
      lds_barrier();
      if (w < 5) {
        v2f st[5];
#pragma unroll
        for (int i = 0; i < 5; ++i) st[i] = stat[i];
        const float mean = (st[0].x + st[1].x + st[2].x + st[3].x + st[4].x) * 0.2f;
        float m2 = 0.f;
#pragma unroll
        for (int i = 0; i < 5; ++i) { const float d = st[i].x - mean; m2 += st[i].y + 128.f * d * d; }
        const float mb = rbf(mean), sd = rbf(sqrtf(rbf(rbf(fmaxf(m2, 0.f) * (1.f / D)) + 1e-5f)));
        const float l0 = rbf(rbf(x20 - mb) / sd), l1 = rbf(rbf(x21 - mb) / sd);
        ((LAS unsigned*)(L + LDS_HB))[pi] = pack2(rbf(l0 * rbf(1.f + lo16(cbp))) + lo16(sbp), rbf(l1 * rbf(1.f + hi16(cbp))) + hi16(sbp));
      }
      lds_barrier();
      if (w == 0) STAMP(7);
      {
        const LAS unsigned* hb = (const LAS unsigned*)(L + LDS_HB) + 5 * rp;
        float a[8];
#pragma unroll
        for (int k = 0; k < 8; ++k) a[k] = 0.f;
#pragma unroll
        for (int i = 0; i < 10; ++i) { const unsigned hv = hb[(i / 5) * 160 + (i % 5)];
#pragma unroll
          for (int k = 0; k < 8; ++k) a[k] = dot2(gu[i][k], hv, a[k]); }
        const float v = rbf(reduce8_over32(a, lane));
        auto sw = __builtin_amdgcn_permlane32_swap(__float_as_uint(v), __float_as_uint(v), false, false);
        const float h2 = __uint_as_float(sw[0]) * __uint_as_float(sw[1]);
        if ((lane & 35) == 0) ((LAS __bf16*)(L + LDS_QS))[8 * w + (lane >> 2)] = (__bf16)h2;
      }
      lds_barrier();
      if (w == 0) STAMP(8);
      if (w < 5) {
        const LAS v4u* hp = (const LAS v4u*)(L + LDS_QS + (lane >> 4) * 32);
        const v4u h0 = hp[0], h1 = hp[1];
        float a[8];
#pragma unroll
        for (int k = 0; k < 8; ++k) a[k] = 0.f;
#pragma unroll
        for (int i = 0; i < 8; ++i) { const unsigned hv = i < 4 ? h0[i & 3] : h1[i & 3];
#pragma unroll
          for (int k = 0; k < 8; ++k) a[k] = dot2(dw[i][k], hv, a[k]); }
        float y[4];
#pragma unroll
        for (int i = 0; i < 4; ++i) y[i] = fold32(a[i], a[i + 4]);
        const float z0 = fold16(y[0], y[2]), z1 = fold16(y[1], y[3]);
        stsc2(v2u{guard(z0), guard(z1)}, rw, WS_DP + (cj * D + 128 * w + 8 * (lane & 15) + 4 * (lane >> 5) + 2 * ((lane >> 4) & 1)) * 4);
        if (w == 0) STAMP(9);
      } else if (w == 5 && cj < 20) {
        const float x2v = ((const LAS float*)(L + LDS_VEC2))[ocol];
        v4u dv[3];
#pragma unroll 1
        for (unsigned sp = 0; sp < SPIN_CAP; ++sp) {
        MEMCLOB();
#pragma unroll
          for (int i = 0; i < 3; ++i) dv[i] = ldsc4(rw, WS_DP + ((r + 8 * i) * D + 32 * cj + 4 * c) * 4);
          if (__all(ok4(dv[0]) & ok4(dv[1]) & ok4(dv[2]))) break;
        }
        STAMP(10);
        const v4f sm = (__builtin_bit_cast(v4f, dv[0]) + __builtin_bit_cast(v4f, dv[1])) + __builtin_bit_cast(v4f, dv[2]);
        float z = fold16(fold32(sm.x, sm.z), fold32(sm.y, sm.w));
        z += dpp<XOR8>(z);
        const float o = x2v + rbf(gbv * rbf(z));
        if ((lane & 8) == 0) ((GAS __bf16*)out)[ocol] = (__bf16)o;
        if (cj == 0 && lane == 0) stsc1(tag, rw, WS_EP);
        STAMP(11);
      }
    }
  }
}

extern "C" void kernel_launch(void* const* d_in, const int* in_sizes, int n_in, void* d_out, int out_size, void* d_ws, size_t ws_size,
                              hipStream_t stream) {
  (void)hipFuncSetAttribute((const void*)mega, hipFuncAttributeMaxDynamicSharedMemorySize, LDS_BYTES);
  hipLaunchKernelGGL(mega, dim3(256), dim3(512), LDS_BYTES, stream, (const u16*)d_in[0], (const u16*)d_in[1], (const u16*)d_in[2], (const u16*)d_in[3],
                     (const u16*)d_in[4], (const u16*)d_in[6], (const u16*)d_in[7], (const u16*)d_in[8], (const u16*)d_in[9], (const u16*)d_in[10],
                     (u16*)d_out, (unsigned char*)d_ws);
}
```

```cpp
#include <hip/hip_runtime.h>
#include <hip/hip_bf16.h>
#include <cstdint>

typedef unsigned short u16;
typedef unsigned v4u __attribute__((ext_vector_type(4)));
typedef unsigned v2u __attribute__((ext_vector_type(2)));
typedef float v4f __attribute__((ext_vector_type(4)));
typedef float v2f __attribute__((ext_vector_type(2)));
typedef __bf16 bf16x2 __attribute__((ext_vector_type(2)));
typedef __amdgpu_buffer_rsrc_t rsrc_t;
#define LAS __attribute__((address_space(3)))
#define GAS __attribute__((address_space(1)))
#define DEV __device__ __forceinline__

#define CP 5
#define SP 2
#define SP2 5
#define LA 3
#define QP 4
#define TRG 2
#define SPV SP
#define KD 11
#define TLBW 0
#define ILV 1
#define KI 2
#define MLT 9
#define SLM 2
#define SLO 1
#define SLW 0
#define PFKB 0
#define WOP 1

constexpr int D = 640, SK = 4096, DH = 192, FF = 1536;
constexpr int AUX_NT = 2, AUX_SC1 = 16;
constexpr int WS_EP = 0, WS_PROG = 2048, WS_MODG = 4096, WS_QG = 32768, WS_MLG = 65536, WS_OP = 131072, WS_WP = 655360, WS_SAG = 98304, WS_DP = 786432, WS_BYTES = 1 << 20;
constexpr unsigned SPIN_CAP = 1u << 17;
constexpr float LOG2E = 1.4426950408889634f;
constexpr float QSCALE = 0.07216878364870322f;

#define STAMP(i) do { } while (0)

constexpr int LDS_RED = 0;
constexpr int LDS_VEC = 6144;
constexpr int LDS_VEC2 = 8704;
constexpr int LDS_HB = 11264;
constexpr int LDS_QS = 12800;
constexpr int LDS_STAMP = 15872;
constexpr int LDS_BYTES = 16896;

DEV float lo16(unsigned w) { return __uint_as_float(w << 16); }
DEV float hi16(unsigned w) { return __uint_as_float(w & 0xffff0000u); }
DEV float f32(unsigned w) { return __uint_as_float(w); }
DEV unsigned u32(float f) { return __float_as_uint(f); }
DEV float rbf(float f) { return (float)(__bf16)f; }
DEV unsigned pack2(float a, float b) { bf16x2 v = {(__bf16)a, (__bf16)b}; return __builtin_bit_cast(unsigned, v); }
DEV float dot2(unsigned a, unsigned b, float acc) { return __builtin_amdgcn_fdot2_f32_bf16(__builtin_bit_cast(bf16x2, a), __builtin_bit_cast(bf16x2, b), acc, false); }
template <int CTRL> DEV float dpp(float x) { return __builtin_bit_cast(float, __builtin_amdgcn_mov_dpp(__builtin_bit_cast(int, x), CTRL, 0xf, 0xf, true)); }
constexpr int XOR1 = 0xB1, XOR2 = 0x4E, XOR7 = 0x141, XOR8 = 0x128, ROR4 = 0x124;
DEV float fold32(float a, float b) { auto s = __builtin_amdgcn_permlane32_swap(u32(a), u32(b), false, false); return f32(s[0]) + f32(s[1]); }
DEV float fold16(float a, float b) { auto s = __builtin_amdgcn_permlane16_swap(u32(a), u32(b), false, false); return f32(s[0]) + f32(s[1]); }
DEV float max32(float a) { auto s = __builtin_amdgcn_permlane32_swap(u32(a), u32(a), false, false); return fmaxf(f32(s[0]), f32(s[1])); }
DEV float max16(float a) { auto s = __builtin_amdgcn_permlane16_swap(u32(a), u32(a), false, false); return fmaxf(f32(s[0]), f32(s[1])); }
DEV float wave_sum(float x) { x += dpp<XOR1>(x); x += dpp<XOR2>(x); x += dpp<XOR7>(x); x += dpp<XOR8>(x); x = fold16(x, x); return fold32(x, x); }
DEV float wave_max(float x) { x = fmaxf(x, dpp<XOR1>(x)); x = fmaxf(x, dpp<XOR2>(x)); x = fmaxf(x, dpp<XOR7>(x)); x = fmaxf(x, dpp<XOR8>(x)); x = max16(x); return max32(x); }
DEV rsrc_t mkrs(const void* p, int bytes) { return __builtin_amdgcn_make_buffer_rsrc((void*)p, 0, bytes, 0x00020000); }
DEV v4u ldnt(rsrc_t rs, int voff, int soff) { return __builtin_amdgcn_raw_buffer_load_b128(rs, voff, soff, AUX_NT); }
#define MEMCLOB() asm volatile("" ::: "memory")
DEV v4u ldsc4(rsrc_t rs, int voff, int soff = 0) { return __builtin_amdgcn_raw_buffer_load_b128(rs, voff, soff, AUX_SC1); }
DEV v2u ldsc2(rsrc_t rs, int voff, int soff = 0) { return __builtin_amdgcn_raw_buffer_load_b64(rs, voff, soff, AUX_SC1); }
DEV unsigned ldsc1(rsrc_t rs, int voff) { return __builtin_amdgcn_raw_buffer_load_b32(rs, voff, 0, AUX_SC1); }
DEV void stsc4(v4u v, rsrc_t rs, int voff) { __builtin_amdgcn_raw_buffer_store_b128(v, rs, voff, 0, AUX_SC1); }
DEV void stsc2(v2u v, rsrc_t rs, int voff) { __builtin_amdgcn_raw_buffer_store_b64(v, rs, voff, 0, AUX_SC1); }
DEV void stsc1(unsigned v, rsrc_t rs, int voff) { __builtin_amdgcn_raw_buffer_store_b32(v, rs, voff, 0, AUX_SC1); }
DEV int moff(bool on, int off) { return on ? off : 0x40000000; }
DEV unsigned bad4(v4u v, unsigned tag) { return (v.y ^ tag) | (v.w ^ tag); }
#define PIN24(a) asm volatile("" : "+v"(a[0]), "+v"(a[1]), "+v"(a[2]), "+v"(a[3]), "+v"(a[4]), "+v"(a[5]), "+v"(a[6]), "+v"(a[7]), "+v"(a[8]), "+v"(a[9]), "+v"(a[10]), "+v"(a[11]), \
  "+v"(a[12]), "+v"(a[13]), "+v"(a[14]), "+v"(a[15]), "+v"(a[16]), "+v"(a[17]), "+v"(a[18]), "+v"(a[19]), "+v"(a[20]), "+v"(a[21]), "+v"(a[22]), "+v"(a[23]))
template <int N> DEV void waitvm() { asm volatile("s_waitcnt vmcnt(%0)" ::"i"(N) : "memory"); }
#define SB() __builtin_amdgcn_sched_barrier(0)
DEV void lds_barrier() { asm volatile("s_waitcnt lgkmcnt(0)" ::: "memory"); __builtin_amdgcn_s_barrier(); asm volatile("" ::: "memory"); }
DEV void lds_fence() { asm volatile("s_waitcnt lgkmcnt(0)" ::: "memory"); }

DEV void pairperm(v4u e, v4u o, unsigned (&p)[8]) {
#pragma unroll
  for (int k = 0; k < 4; ++k) { p[2 * k] = __builtin_amdgcn_perm(o[k], e[k], 0x05040100u); p[2 * k + 1] = __builtin_amdgcn_perm(o[k], e[k], 0x07060302u); }
  asm volatile("" : "+v"(p[0]), "+v"(p[1]), "+v"(p[2]), "+v"(p[3]), "+v"(p[4]), "+v"(p[5]), "+v"(p[6]), "+v"(p[7]));
}
DEV void fma8(float (&acc)[24], int base, float p, v4u v) {
#pragma unroll
  for (int k = 0; k < 4; ++k) { acc[base + 2 * k] = fmaf(p, lo16(v[k]), acc[base + 2 * k]); acc[base + 2 * k + 1] = fmaf(p, hi16(v[k]), acc[base + 2 * k + 1]); }
}
DEV void unit_prep(const v4u (&raw)[10], unsigned (&p)[5][8]) {
#pragma unroll
  for (int i = 0; i < 5; ++i) pairperm(raw[2 * i], raw[2 * i + 1], p[i]);
}
DEV void unit_partial(const unsigned (&p)[5][8], LAS unsigned char* L, int vecoff, int redoff, int w, int lane) {
  const LAS unsigned* sc = (const LAS unsigned*)(L + vecoff) + 40 * w + (lane >> 3);
  float a[8];
#pragma unroll
  for (int k = 0; k < 8; ++k) a[k] = 0.f;
#pragma unroll
  for (int i = 0; i < 5; ++i) { const unsigned hv = sc[8 * i];
#pragma unroll
    for (int k = 0; k < 8; ++k) a[k] = dot2(p[i][k], hv, a[k]); }
  float y[4], z[2];
#pragma unroll
  for (int i = 0; i < 4; ++i) y[i] = fold32(a[i], a[i + 4]);
#pragma unroll
  for (int i = 0; i < 2; ++i) z[i] = fold16(y[i], y[i + 2]);
  const float t0 = z[0] + dpp<XOR8>(z[0]), t1 = z[1] + dpp<XOR8>(z[1]);
  ((LAS float*)(L + redoff))[w * 64 + (lane & 7) * 8 + (lane >> 3)] = (lane & 8) ? t1 : t0;
}
DEV v4u ldp(rsrc_t rs, int voff, int soff) { waitvm<CP>(); const v4u v = ldnt(rs, voff, soff); SB(); return v; }
DEV void unit_publish(LAS unsigned char* L, int redoff, int lane, int outoff, unsigned tag, rsrc_t rw) {
  const LAS v2f* red = (const LAS v2f*)(L + redoff) + (lane & 31);
  v2f s = red[0];
#pragma unroll
  for (int i = 1; i < 8; ++i) s += red[i * 32];
  stsc2(v2u{pack2(s.x, s.y), tag}, rw, moff(lane < 32, outoff + lane * 8));
}
template <int LO, int HI, bool PACED> DEV void unit_load(v4u (&raw)[10], const u16* wmod, const u16* wq, bool is_mod, int g, bool on, int w, int lane) {
  const int stride = is_mod ? 6 * D * 2 : 32 * DH * 2;
  const rsrc_t rs = mkrs((const unsigned char*)(is_mod ? wmod : wq) + (size_t)80 * w * stride, on ? 80 * stride : 0);
  const int vo = (lane >> 3) * 2 * stride + g * 128 + (lane & 7) * 16;
#pragma unroll
  for (int i = LO; i < HI; ++i) raw[i] = (PACED ? ldp(rs, vo, (16 * (i >> 1) + (i & 1)) * stride) : ldnt(rs, vo, (16 * (i >> 1) + (i & 1)) * stride));
  SB();
}

__global__ void __launch_bounds__(512, 2)
mega(const u16* x, const u16* Kc, const u16* Vc, const u16* wq, const u16* wo,
     const u16* wg, const u16* wu, const u16* wd, const u16* cvec, const u16* wmod,
     u16* out, unsigned char* ws) {
  __shared__ __attribute__((aligned(16))) unsigned char smem[LDS_BYTES];
  LAS unsigned char* L = (LAS unsigned char*)smem;
  const int b = blockIdx.x, tid = threadIdx.x, lane = tid & 63, w = __builtin_amdgcn_readfirstlane(tid >> 6);
  const int r = lane >> 3, c = lane & 7;
  v4u pf[3];
  {
    const rsrc_t rc = mkrs((const void*)(__builtin_amdgcn_s_getpc() & ~127ull), b < 64 ? PFKB * 1024 : 0);
#pragma unroll
    for (int i = 0; i < 3; ++i) pf[i] = __builtin_amdgcn_raw_buffer_load_b128(rc, tid * 16, i * 8192, 0);
    SB();
  }
  asm volatile("" ::"s"(x), "s"(Kc), "s"(Vc), "s"(wq), "s"(wo), "s"(wg), "s"(wu), "s"(wd), "s"(cvec), "s"(wmod), "s"(out), "s"(ws));
  const rsrc_t rw = mkrs(ws, WS_BYTES);
  if (__builtin_expect(b >= 64, 1)) {
    const int xg = b & 7, ii = (b - 64) >> 3, hq = ii / 6, s = ii - hq * 6, h = xg * 4 + hq;
    const int rem = s < 2 ? 6 : 5, t0cu = 85 * s + (s < 2 ? s : 2), ex = w - (8 - rem);
    const int nt = 10 + (ex >= 0 ? 1 : 0), tw0 = t0cu + 7 - w, TS = 8 * 3072, nrec = (80 + rem - 7 + w) * 3072;
    const size_t kvoff = ((size_t)h * SK + (size_t)tw0 * 8) * DH;
    const rsrc_t rk = mkrs(Kc + kvoff, nrec), rv = mkrs(Vc + kvoff, nrec);
    const int hp = ii >= 10 ? 1 : 0, cg = ii - 10 * hp;
    const rsrc_t rwo = mkrs(wo + (size_t)(768 * xg + 384 * hp + 48 * w) * D + 64 * cg, ii < 20 ? 48 * D * 2 - 128 * cg : 0);
    const int voff = r * 384 + c * 16, l48 = lane < 48 ? lane : 47;
    v4u kr[11][3], vr[12][3];
    unsigned tag, tw;
#define TRIG(t) do { if ((t) == TRG && ii == 23 && w == 0) stsc1(tag, rw, moff(lane == 0, WS_PROG + xg * 128)); } while (0)
#define LDV(t) do { waitvm<SPV>(); _Pragma("unroll") for (int j = 0; j < 3; ++j) vr[t][j] = ldnt(rv, voff + j * 128, (t) * TS); TRIG(t); SB(); } while (0)
#define LDVN(t) do { waitvm<SP2>(); _Pragma("unroll") for (int j = 0; j < 3; ++j) vr[t][j] = ldnt(rv, voff + j * 128, (t) * TS); TRIG(t); SB(); } while (0)
#define VPERM(u) do { _Pragma("unroll") for (int j = 0; j < 3; ++j) pairperm(vr[2 * (u)][j], vr[2 * (u) + 1][j], vp[u][j]); SB(); } while (0)
#define PV(u) do { _Pragma("unroll") for (int j = 0; j < 3; ++j) _Pragma("unroll") for (int k = 0; k < 8; ++k) acc[j * 8 + k] = dot2(vp[u][j][k], pp[u], acc[j * 8 + k]); PIN24(acc); SB(); } while (0)
#pragma unroll
    for (int t = 0; t < 11; ++t) {
      if (t >= KD) waitvm<SPV>(); else if (t >= KI) waitvm<SP>();
      if (t == 3) STAMP(9);
      if (t == 8) tag = ldsc1(rw, WS_EP);
      if (TLBW && t == 1) {
        const u16* tp = (lane & 3) == 0 ? Kc + kvoff + nt * 1536 - 2 : ((lane & 3) == 1 ? Vc + kvoff : ((lane & 3) == 2 ? Vc + kvoff + nt * 1536 - 2 : wo + (size_t)(768 * xg + 384 * hp + 48 * w) * D));
        tw = __builtin_nontemporal_load((const GAS unsigned*)tp);
      }
      if (TLBW && t == 4) asm volatile("" ::"v"(tw));
      if (t == KI) asm volatile("" ::"v"(pf[0]), "v"(pf[1]), "v"(pf[2]));
#pragma unroll
      for (int j = 0; j < 3; ++j) { kr[t][j] = ldnt(rk, voff + j * 128, t * TS); SB(); }
    }
    unsigned vp[6][3][8];
    v4u qg;
    static_assert(QP >= 1 && QP <= 5, "");
#pragma unroll
    for (int t = 0; t <= QP + 1; ++t) {
      if (t == QP) { qg = ldsc4(rw, WS_QG + h * 768 + l48 * 16); SB(); }
      LDV(t);
      if (t == 0) { tag += 1u; asm volatile("" : "+v"(tag)); SB(); }
      if (t == 3) VPERM(0);
      if (t == 5) VPERM(1);
    }
    STAMP(1);
    if (__any(bad4(qg, tag) != 0u)) {
#pragma unroll 1
      for (unsigned sp = 0; sp < SPIN_CAP; ++sp) {
        MEMCLOB();
        qg = ldsc4(rw, WS_QG + h * 768 + l48 * 16);
        if (!__any(bad4(qg, tag) != 0u)) break;
        __builtin_amdgcn_s_sleep(2);
      }
    }
    STAMP(2);
    LAS unsigned* qs = (LAS unsigned*)(L + LDS_QS) + w * 96;
    *(LAS v2u*)(qs + 2 * l48) = v2u{qg.x, qg.z};
    lds_fence();
    v4u qp[3];
#pragma unroll
    for (int j = 0; j < 3; ++j) qp[j] = *(const LAS v4u*)(qs + j * 32 + c * 4);
    float sc[12];
#pragma unroll
    for (int t = 0; t < 11; ++t) {
      float d = 0.f;
#pragma unroll
      for (int j = 0; j < 3; ++j)
#pragma unroll
        for (int k = 0; k < 4; ++k) d = dot2(kr[t][j][k], qp[j][k], d);
      d += dpp<XOR1>(d); d += dpp<XOR2>(d); d += dpp<XOR7>(d);
      d = rbf(rbf(d) * QSCALE);
      sc[t] = (t < 10 || nt == 11) ? d : -INFINITY;
      asm volatile("" : "+v"(sc[t]));
      if (t == 4) { SB(); LDVN(QP + 2); }
      if (t == 10) { SB(); LDVN(QP + 3); }
    }
    float mw = sc[0];
#pragma unroll
    for (int t = 1; t < 11; ++t) mw = fmaxf(mw, sc[t]);
    mw = fmaxf(mw, dpp<XOR8>(mw)); mw = max16(mw); mw = max32(mw);
    float lw = 0.f;
#pragma unroll
    for (int t = 0; t < 11; ++t) { sc[t] = __builtin_amdgcn_exp2f((sc[t] - mw) * LOG2E); lw += sc[t]; }
    sc[11] = 0.f;
    lw += dpp<XOR8>(lw); lw = fold16(lw, lw); lw = fold32(lw, lw);
    unsigned pp[6];
#pragma unroll
    for (int u = 0; u < 6; ++u) pp[u] = pack2(sc[2 * u], sc[2 * u + 1]);
    ((LAS v2f*)(L + LDS_HB))[w] = v2f{mw, lw};
    float acc[24];
#pragma unroll
    for (int i = 0; i < 24; ++i) acc[i] = 0.f;
    if (QP + 1 < 3) VPERM(0);
    PV(0);
    v4u wr[6];
#define LDWO() do { _Pragma("unroll") for (int i = 0; i < 6; ++i) wr[i] = ldnt(rwo, r * (2 * D * 2) + c * 16, (16 * (i >> 1) + (i & 1)) * D * 2); SB(); } while (0)
    float fw;
    static_assert(MLT >= QP + 4 && MLT <= 10, "");
#pragma unroll
    for (int t = QP + 4; t <= 10; ++t) {
      LDVN(t);
      if (t == MLT) {
        lds_barrier();
        STAMP(3);
        const v2f ml = ((const LAS v2f*)(L + LDS_HB))[c];
        float M = ml.x; M = fmaxf(M, dpp<XOR1>(M)); M = fmaxf(M, dpp<XOR2>(M)); M = fmaxf(M, dpp<XOR7>(M));
        float Lc = ml.y * __builtin_amdgcn_exp2f((ml.x - M) * LOG2E); Lc += dpp<XOR1>(Lc); Lc += dpp<XOR2>(Lc); Lc += dpp<XOR7>(Lc);
        fw = __builtin_amdgcn_exp2f((mw - M) * LOG2E);
        asm volatile("" : "+v"(fw));
        if (w == 0) stsc4(v4u{u32(M), tag, u32(Lc), tag}, rw, moff(lane == 0, WS_MLG + h * 128 + s * 16));
        SB();
      }
      if (t == QP + 4) { if (QP + 1 < 5) VPERM(1); PV(1); }
      if (t == (QP + 4 > 7 ? QP + 4 : 7)) { VPERM(2); PV(2); }
      if (t == 9) { VPERM(3); PV(3); }
    }
    if (WOP == 0) LDWO();
    VPERM(4); PV(4);
    if (WOP == 1) LDWO();
#pragma unroll
    for (int j = 0; j < 3; ++j) fma8(acc, j * 8, lo16(pp[5]), vr[10][j]);
    PIN24(acc); SB();
    if (WOP == 2) LDWO();
    STAMP(4);
    {
      float y[12], z[6];
#pragma unroll
      for (int i = 0; i < 12; ++i) y[i] = fold32(acc[i], acc[i + 12]);
#pragma unroll
      for (int i = 0; i < 6; ++i) z[i] = fold16(y[i], y[i + 6]);
      LAS float* red = (LAS float*)(L + LDS_RED) + w * 192 + c * 24 + r * 3;
#pragma unroll
      for (int i = 0; i < 3; ++i) { const float t0 = z[i] + dpp<XOR8>(z[i]), t1 = z[i + 3] + dpp<XOR8>(z[i + 3]); red[i] = ((lane & 8) ? t1 : t0) * fw; }
    }
    const int hh = 4 * xg + 2 * hp + (w >> 2), mloff = WS_MLG + hh * 128 + (c < 6 ? c : 5) * 16;
    v4u ml = ldsc4(rw, mloff);
    lds_barrier();
    if (w < 2) {
      const int dp = 48 * w + l48;
      const LAS v2f* red = (const LAS v2f*)(L + LDS_RED) + ((dp >> 2) & 7) * 12 + (dp >> 5) * 4 + (dp & 3);
      v2f t[8];
#pragma unroll
      for (int i = 0; i < 8; ++i) t[i] = red[i * 96];
      const v2f o = ((t[0] + t[1]) + (t[2] + t[3])) + ((t[4] + t[5]) + (t[6] + t[7]));
      stsc4(v4u{u32(o.x), tag, u32(o.y), tag}, rw, moff(lane < 48, WS_OP + (h * 8 + s) * 2048 + dp * 16));
    }
    STAMP(5);
    if (ii < 20) {
      unsigned wp[3][8];
      if (__any(bad4(ml, tag) != 0u)) {
#pragma unroll 1
        for (unsigned sp = 0; sp < SPIN_CAP; ++sp) {
          MEMCLOB();
          ml = ldsc4(rw, mloff);
          if (!__any(bad4(ml, tag) != 0u)) break;
          __builtin_amdgcn_s_sleep(SLM);
        }
      }
      if (WOP == 3) LDWO();
      float wsp[6];
      {
        float M = f32(ml.x); M = fmaxf(M, dpp<XOR1>(M)); M = fmaxf(M, dpp<XOR2>(M)); M = fmaxf(M, dpp<XOR7>(M));
        const float e = __builtin_amdgcn_exp2f((f32(ml.x) - M) * LOG2E);
        float Ls = c < 6 ? e * f32(ml.z) : 0.f; Ls += dpp<XOR1>(Ls); Ls += dpp<XOR2>(Ls); Ls += dpp<XOR7>(Ls);
        const float wv = e / Ls;
#pragma unroll
        for (int i = 0; i < 6; ++i) wsp[i] = __builtin_bit_cast(float, __builtin_amdgcn_readlane(__builtin_bit_cast(int, wv), i));
      }
#pragma unroll
      for (int i = 0; i < 3; ++i) pairperm(wr[2 * i], wr[2 * i + 1], wp[i]);
      STAMP(6);
      const int hf = lane >> 5, p24 = (lane & 31) < 24 ? (lane & 31) : 23;
      const int opoff = WS_OP + (hh * 8 + 3 * hf) * 2048 + (24 * (w & 3) + p24) * 16;
      const float w0 = hf ? wsp[3] : wsp[0], w1 = hf ? wsp[4] : wsp[1], w2 = hf ? wsp[5] : wsp[2];
      v4u op[3];
#pragma unroll 1
      for (unsigned sp = 0; sp < SPIN_CAP; ++sp) {
        MEMCLOB();
#pragma unroll
        for (int i = 0; i < 3; ++i) op[i] = ldsc4(rw, opoff, i * 2048);
        if (!__any((bad4(op[0], tag) | bad4(op[1], tag) | bad4(op[2], tag)) != 0u)) break;
        __builtin_amdgcn_s_sleep(SLO);
      }
      STAMP(7);
      float o0 = fmaf(w2, f32(op[2].x), fmaf(w1, f32(op[1].x), w0 * f32(op[0].x))), o1 = fmaf(w2, f32(op[2].z), fmaf(w1, f32(op[1].z), w0 * f32(op[0].z)));
      o0 = fold32(o0, o0); o1 = fold32(o1, o1);
      const int opk = (int)pack2(o0, o1);
      unsigned ov[3];
#pragma unroll
      for (int i = 0; i < 3; ++i) ov[i] = (unsigned)__builtin_amdgcn_ds_bpermute((8 * i + r) * 4, opk);
      float a[8];
#pragma unroll
      for (int k = 0; k < 8; ++k) a[k] = dot2(wp[2][k], ov[2], dot2(wp[1][k], ov[1], dot2(wp[0][k], ov[0], 0.f)));
      float y[4], z[2];
#pragma unroll
      for (int i = 0; i < 4; ++i) y[i] = fold32(a[i], a[i + 4]);
#pragma unroll
      for (int i = 0; i < 2; ++i) { z[i] = fold16(y[i], y[i + 2]); z[i] += dpp<XOR8>(z[i]); }
      ((LAS float*)(L + LDS_VEC))[w * 64 + c * 8 + r] = (lane & 8) ? z[1] : z[0];
      lds_barrier();
      if (w == 2) {
        const LAS v2f* red = (const LAS v2f*)(L + LDS_VEC) + (lane & 31);
        v2f t[8];
#pragma unroll
        for (int i = 0; i < 8; ++i) t[i] = red[i * 32];
        const v2f o = ((t[0] + t[1]) + (t[2] + t[3])) + ((t[4] + t[5]) + (t[6] + t[7]));
        stsc4(v4u{u32(o.x), tag, u32(o.y), tag}, rw, moff(lane < 32, WS_WP + ((2 * xg + hp) * D + 64 * cg + 2 * lane) * 8));
      }
      STAMP(8);
    }
  } else {
    const int xg = b & 7, v = b >> 3;
    const int t320 = tid < 320 ? tid : 319;
    unsigned tag, cpair;
#define LDC() do { MEMCLOB(); cpair = ((const GAS unsigned*)cvec)[t320]; tag = ldsc1(rw, WS_EP); SB(); } while (0)
    unsigned xpair, xall[5];
    float ln0, ln1;
    v4u u0[10], u1[10], sg[4];
    unsigned p0[5][8], p1[5][8];
    const int rp = lane & 31, hf = lane >> 5;
#define FASTG(p) ((p) + ((p) + 4) / 7)
#define LDX() do { xpair = ((const GAS unsigned*)x)[t320]; _Pragma("unroll") for (int i = 0; i < 5; ++i) xall[i] = ((const GAS unsigned*)x)[lane + 64 * i]; SB(); } while (0)
#define SILU() do { asm volatile("" ::"v"(pf[0]), "v"(pf[1]), "v"(pf[2])); tag += 1u; const float c0 = lo16(cpair), c1 = hi16(cpair); \
      ((LAS unsigned*)(L + LDS_VEC))[t320] = pack2(c0 / (1.f + expf(-c0)), c1 / (1.f + expf(-c1))); SB(); } while (0)
#define LNX() do { float sm = 0.f; \
      _Pragma("unroll") for (int i = 0; i < 5; ++i) sm += lo16(xall[i]) + hi16(xall[i]); \
      const float mean = wave_sum(sm) * (1.f / D); float sq = 0.f; \
      _Pragma("unroll") for (int i = 0; i < 5; ++i) { const float a = lo16(xall[i]) - mean, bb = hi16(xall[i]) - mean; sq += a * a + bb * bb; } \
      const float var = wave_sum(sq) * (1.f / D); \
      const float mb = rbf(mean), sd = rbf(sqrtf(rbf(rbf(var) + 1e-5f))); \
      ln0 = rbf(rbf(lo16(xpair) - mb) / sd); ln1 = rbf(rbf(hi16(xpair) - mb) / sd); \
      asm volatile("" : "+v"(ln0), "+v"(ln1)); SB(); } while (0)
#define LDSAG() do { _Pragma("unroll") for (int i = 0; i < 4; ++i) sg[i] = ldsc4(rw, moff(tid < 320, WS_SAG + tid * 16), (i & 1) * 10240 + (i >> 1) * 5120); } while (0)
#define MAKEH() do { if (w < 5) { \
      if (__any((bad4(sg[0], tag) | bad4(sg[1], tag) | bad4(sg[2], tag) | bad4(sg[3], tag)) != 0u)) { \
        _Pragma("unroll 1") for (unsigned sp = 0; sp < SPIN_CAP; ++sp) { \
          MEMCLOB(); LDSAG(); \
          if (!__any((bad4(sg[0], tag) | bad4(sg[1], tag) | bad4(sg[2], tag) | bad4(sg[3], tag)) != 0u)) break; \
          __builtin_amdgcn_s_sleep(1); } } \
      const float sa0 = rbf(f32(sg[0].x) + f32(sg[1].x)), sa1 = rbf(f32(sg[0].z) + f32(sg[1].z)); \
      const float ca0 = rbf(f32(sg[2].x) + f32(sg[3].x)), ca1 = rbf(f32(sg[2].z) + f32(sg[3].z)); \
      ((LAS unsigned*)(L + LDS_VEC2))[tid] = pack2(rbf(ln0 * rbf(1.f + ca0)) + sa0, rbf(ln1 * rbf(1.f + ca1)) + sa1); } \
      STAMP(2); lds_barrier(); } while (0)
    if (b < 40) {
      const int wi = 5 * xg + v, ga_ = wi < 20 ? wi : wi - 20, ha_ = wi < 20 ? 0 : 1, uu = b - 8;
      const bool m1 = !(v == 0 || (uu >= 12 && uu < 18));
      const int g0 = v == 0 ? FASTG(20 + xg) : (uu < 12 ? 8 * uu + 3 : FASTG(uu - 12));
      const int g1 = v == 0 ? FASTG(28 + xg) : (uu < 12 ? 20 + uu : (uu < 18 ? FASTG(24 + uu) : 14 + uu));
      v4u ua[6];
      LDC();
      LDX();
      {
        const rsrc_t ra = mkrs(wmod + (size_t)(320 * ha_ + 40 * w) * (6 * D), 40 * 6 * D * 2);
#pragma unroll
        for (int i = 0; i < 6; ++i) ua[i] = ldnt(ra, 2 * r * (6 * D * 2) + ga_ * 128 + c * 16, (16 * (i >> 1) + (i & 1)) * (6 * D * 2));
        SB();
      }
      SILU();
      lds_barrier();
      unsigned hv[3];
      {
        const LAS unsigned* sc = (const LAS unsigned*)(L + LDS_VEC) + 160 * ha_ + 20 * w + r;
        hv[0] = sc[0]; hv[1] = sc[8]; hv[2] = sc[16]; hv[2] = r < 4 ? hv[2] : 0u;
      }
      STAMP(8);
      unit_load<0, 5, true>(u0, wmod, wq, false, g0, true, w, lane);
      STAMP(9);
      {
        unsigned pa[3][8];
#pragma unroll
        for (int i = 0; i < 3; ++i) pairperm(ua[2 * i], ua[2 * i + 1], pa[i]);
        float a[8];
#pragma unroll
        for (int k = 0; k < 8; ++k) a[k] = dot2(pa[2][k], hv[2], dot2(pa[1][k], hv[1], dot2(pa[0][k], hv[0], 0.f)));
        float y[4], z[2];
#pragma unroll
        for (int i = 0; i < 4; ++i) y[i] = fold32(a[i], a[i + 4]);
#pragma unroll
        for (int i = 0; i < 2; ++i) z[i] = fold16(y[i], y[i + 2]);
        const float t0 = z[0] + dpp<XOR8>(z[0]), t1 = z[1] + dpp<XOR8>(z[1]);
        ((LAS float*)(L + LDS_RED))[w * 64 + c * 8 + r] = (lane & 8) ? t1 : t0;
        lds_barrier();
        if (w == 0) {
          const LAS v2f* red = (const LAS v2f*)(L + LDS_RED) + (lane & 31);
          v2f sm = red[0];
#pragma unroll
          for (int i = 1; i < 8; ++i) sm += red[i * 32];
          stsc4(v4u{u32(sm.x), tag, u32(sm.y), tag}, rw, moff(lane < 32, WS_SAG + ha_ * 10240 + ga_ * 512 + lane * 16));
        }
      }
      STAMP(1);
      SB();
      unit_load<5, 10, true>(u0, wmod, wq, false, g0, true, w, lane);
      LNX();
      waitvm<2>();
      LDSAG();
      SB();
      unit_load<0, LA, false>(u1, wmod, wq, m1, g1, true, w, lane);
      unit_prep(u0, p0);
      SB();
      MAKEH();
      unit_partial(p0, L, LDS_VEC2, LDS_RED + 2048, w, lane);
      lds_barrier();
      if (w == 0) unit_publish(L, LDS_RED + 2048, lane, WS_QG + g0 * 256, tag, rw);
      STAMP(13);
      SB();
      unit_load<LA, 10, true>(u1, wmod, wq, m1, g1, true, w, lane);
      unit_prep(u1, p1);
      unit_partial(p1, L, m1 ? LDS_VEC : LDS_VEC2, LDS_RED + 4096, w, lane);
      lds_barrier();
      if (w == 0) unit_publish(L, LDS_RED + 4096, lane, (m1 ? WS_MODG : WS_QG) + g1 * 256, tag, rw);
      STAMP(3);
      SB();
      if (uu >= 18) {
        unit_load<0, 10, true>(u0, wmod, wq, true, 28 + uu, true, w, lane);
        unit_prep(u0, p0);
        unit_partial(p0, L, LDS_VEC, LDS_RED, w, lane);
        lds_barrier();
        if (w == 0) unit_publish(L, LDS_RED, lane, WS_MODG + (28 + uu) * 256, tag, rw);
      }
    } else {
      const int f = 3 * xg + v - 5, fi = f - ((f + 4) >> 3);
      const int g0 = FASTG(42 + fi), g1 = FASTG(63 + fi);
      LDC();
      LDX();
      if ((f & 7) != 3) {
        unit_load<0, 6, false>(u0, wmod, wq, false, g0, true, w, lane);
        tag += 1u;
        asm volatile("" ::"v"(pf[0]), "v"(pf[1]), "v"(pf[2]), "v"(cpair));
        unit_load<6, 10, true>(u0, wmod, wq, false, g0, true, w, lane);
        LNX();
        STAMP(1);
        unit_load<0, 8, true>(u1, wmod, wq, false, g1, true, w, lane);
        unit_prep(u0, p0);
        unit_load<8, 10, true>(u1, wmod, wq, false, g1, true, w, lane);
        waitvm<2>();
        LDSAG();
        SB();
        unit_prep(u1, p1);
        SB();
        MAKEH();
        unit_partial(p0, L, LDS_VEC2, LDS_RED + 2048, w, lane);
        unit_partial(p1, L, LDS_VEC2, LDS_RED + 4096, w, lane);
        lds_barrier();
        if (w < 2) unit_publish(L, LDS_RED + 2048 + w * 2048, lane, WS_QG + (w ? g1 : g0) * 256, tag, rw);
        STAMP(3);
        SB();
#pragma unroll 1
        for (unsigned sp = 0; sp < SPIN_CAP; ++sp) {
          MEMCLOB();
          if (__builtin_amdgcn_readfirstlane(ldsc1(rw, WS_PROG + xg * 128) ^ tag) == 0u) break;
          __builtin_amdgcn_s_sleep(8);
        }
      } else {
        tag += 1u;
        asm volatile("" ::"v"(pf[0]), "v"(pf[1]), "v"(pf[2]), "v"(cpair));
      }
      STAMP(5);
      SB();
      v4u dr[16];
      unsigned dw[8][8];
      {
        const rsrc_t rg = mkrs(wg + (size_t)80 * w * FF + f * 64, 80 * FF * 2 - f * 128), ru = mkrs(wu + (size_t)80 * w * FF + f * 64, 80 * FF * 2 - f * 128);
        const rsrc_t rd = mkrs(wd + (size_t)f * 64 * D + w * 128, w < 5 ? 64 * D * 2 - w * 256 : 0);
#pragma unroll
        for (int i = 0; i < 10; ++i) u0[i] = ldp(rg, 2 * r * (FF * 2) + c * 16, (16 * (i >> 1) + (i & 1)) * (FF * 2));
#pragma unroll
        for (int i = 0; i < 10; ++i) u1[i] = ldp(ru, 2 * r * (FF * 2) + c * 16, (16 * (i >> 1) + (i & 1)) * (FF * 2));
        unit_prep(u0, p0);
#pragma unroll
        for (int i = 0; i < 16; ++i) { waitvm<9>(); dr[i] = ldnt(rd, (lane >> 4) * (16 * D * 2) + (lane & 15) * 16, i * D * 2); SB(); }
      }
      unit_prep(u1, p1);
      const bool red_on = (w == 5 || w == 6) && f < 20;
      const int opr = 16 * f + 8 * (w - 5) + c;
      float ga0, ga1, sb0, sb1, cb0, cb1;
      {
        v2u a, bb, cc;
        const int o1 = w < 5 ? WS_MODG + (640 + tid) * 8 : WS_MODG + (1600 + (red_on ? opr : 0)) * 8, o2 = w < 5 ? 2560 : 0;
#pragma unroll 1
        for (unsigned sp = 0; sp < SPIN_CAP; ++sp) {
          MEMCLOB();
          a = ldsc2(rw, o1); bb = ldsc2(rw, o1 + o2); cc = ldsc2(rw, o1 + 2 * o2);
          if (!__any(((a.y ^ tag) | (bb.y ^ tag) | (cc.y ^ tag)) != 0u)) break;
          __builtin_amdgcn_s_sleep(4);
        }
        ga0 = lo16(a.x); ga1 = hi16(a.x); sb0 = lo16(bb.x); sb1 = hi16(bb.x); cb0 = rbf(1.f + lo16(cc.x)); cb1 = rbf(1.f + hi16(cc.x));
      }
#pragma unroll
      for (int i = 0; i < 8; ++i) pairperm(dr[2 * i], dr[2 * i + 1], dw[i]);
      STAMP(4);
      LAS v2f* stat = (LAS v2f*)(L + LDS_QS + 1024);
      float x20 = 0.f, x21 = 0.f;
      if (w < 5) {
        v4u pv[16];
        unsigned miss = 0xffu;
#pragma unroll 1
        for (unsigned sp = 0; sp < SPIN_CAP && miss; ++sp) {
          MEMCLOB();
          if (SLW && sp) __builtin_amdgcn_s_sleep(SLW);
#pragma unroll
          for (int g = 0; g < 8; ++g) if (miss & (1u << g)) { pv[2 * g] = ldsc4(rw, WS_WP + tid * 16, (2 * g) * D * 8); pv[2 * g + 1] = ldsc4(rw, WS_WP + tid * 16, (2 * g + 1) * D * 8); }
#pragma unroll
          for (int g = 0; g < 8; ++g) if (miss & (1u << g)) {
            if (!__any((bad4(pv[2 * g], tag) | bad4(pv[2 * g + 1], tag)) != 0u)) {
              miss &= ~(1u << g);
              pv[2 * g].x = u32(f32(pv[2 * g].x) + f32(pv[2 * g + 1].x)); pv[2 * g].z = u32(f32(pv[2 * g].z) + f32(pv[2 * g + 1].z));
            }
          }
        }
        STAMP(6);
        const v2f at = {((f32(pv[0].x) + f32(pv[2].x)) + (f32(pv[4].x) + f32(pv[6].x))) + ((f32(pv[8].x) + f32(pv[10].x)) + (f32(pv[12].x) + f32(pv[14].x))),
                        ((f32(pv[0].z) + f32(pv[2].z)) + (f32(pv[4].z) + f32(pv[6].z))) + ((f32(pv[8].z) + f32(pv[10].z)) + (f32(pv[12].z) + f32(pv[14].z)))};
        x20 = rbf(lo16(xpair) + rbf(ga0 * rbf(at.x))); x21 = rbf(hi16(xpair) + rbf(ga1 * rbf(at.y)));
        *(LAS v2f*)(L + LDS_VEC2 + tid * 8) = v2f{x20, x21};
        const float piv = __builtin_bit_cast(float, __builtin_amdgcn_readfirstlane(__builtin_bit_cast(int, x20)));
        const float d0 = x20 - piv, d1 = x21 - piv;
        const float s1 = wave_sum(d0 + d1), s2 = wave_sum(d0 * d0 + d1 * d1);
        stat[w] = v2f{piv + s1 * (1.f / 128), s2 - s1 * s1 * (1.f / 128)};
      }
      lds_barrier();
      if (w < 5) {
        v2f st[5];
#pragma unroll
        for (int i = 0; i < 5; ++i) st[i] = stat[i];
        const float mean = (st[0].x + st[1].x + st[2].x + st[3].x + st[4].x) * 0.2f;
        float m2 = 0.f;
#pragma unroll
        for (int i = 0; i < 5; ++i) { const float d = st[i].x - mean; m2 += st[i].y + 128.f * d * d; }
        const float mb = rbf(mean), sd = rbf(__builtin_amdgcn_sqrtf(rbf(rbf(fmaxf(m2, 0.f) * (1.f / D)) + 1e-5f)));
        const float rs = __builtin_amdgcn_rcpf(sd);
        const float l0 = rbf(rbf(x20 - mb) * rs), l1 = rbf(rbf(x21 - mb) * rs);
        ((LAS unsigned*)(L + LDS_HB))[tid] = pack2(rbf(l0 * cb0) + sb0, rbf(l1 * cb1) + sb1);
      }
      lds_barrier();
      STAMP(7);
      unit_partial(p0, L, LDS_HB, LDS_RED, w, lane);
      unit_partial(p1, L, LDS_HB, LDS_RED + 2048, w, lane);
      lds_barrier();
      STAMP(8);
      if (w < 5) {
        const LAS float* red = (const LAS float*)(L + LDS_RED) + lane;
        float t[16];
#pragma unroll
        for (int i = 0; i < 16; ++i) t[i] = red[i * 64];
        const float gv = rbf(((t[0] + t[1]) + (t[2] + t[3])) + ((t[4] + t[5]) + (t[6] + t[7])));
        const float uv = rbf(((t[8] + t[9]) + (t[10] + t[11])) + ((t[12] + t[13]) + (t[14] + t[15])));
        ((LAS __bf16*)(L + LDS_QS + w * 128))[lane] = (__bf16)(gv * uv);
        lds_fence();
        const LAS v4u* hp2 = (const LAS v4u*)(L + LDS_QS + w * 128 + (lane >> 4) * 32);
        const v4u h0 = hp2[0], h1 = hp2[1];
        float a[8];
#pragma unroll
        for (int k = 0; k < 8; ++k) a[k] = 0.f;
#pragma unroll
        for (int i = 0; i < 8; ++i) { const unsigned hv = i < 4 ? h0[i & 3] : h1[i & 3];
#pragma unroll
          for (int k = 0; k < 8; ++k) a[k] = dot2(dw[i][k], hv, a[k]); }
        float y[4];
#pragma unroll
        for (int i = 0; i < 4; ++i) y[i] = fold32(a[i], a[i + 4]);
        const float z0 = fold16(y[0], y[2]), z1 = fold16(y[1], y[3]);
        stsc4(v4u{u32(z0), tag, u32(z1), tag}, rw, WS_DP + (f * D + 128 * w + 8 * (lane & 15) + 4 * hf + 2 * ((lane >> 4) & 1)) * 8);
        STAMP(9);
      } else if (red_on) {
        const v2f x2v = ((const LAS v2f*)(L + LDS_VEC2))[opr];
        v4u dv[3];
#pragma unroll 1
        for (unsigned sp = 0; sp < SPIN_CAP; ++sp) {
          MEMCLOB();
#pragma unroll
          for (int i = 0; i < 3; ++i) dv[i] = ldsc4(rw, WS_DP + r * (D * 8) + opr * 16, i * 8 * D * 8);
          if (!__any((bad4(dv[0], tag) | bad4(dv[1], tag) | bad4(dv[2], tag)) != 0u)) break;
        }
        STAMP(10);
        float z0 = (f32(dv[0].x) + f32(dv[1].x)) + f32(dv[2].x), z1 = (f32(dv[0].z) + f32(dv[1].z)) + f32(dv[2].z);
        z0 += dpp<XOR8>(z0); z1 += dpp<XOR8>(z1);
        z0 = fold16(z0, z0); z1 = fold16(z1, z1); z0 = fold32(z0, z0); z1 = fold32(z1, z1);
        const unsigned o = pack2(x2v.x + rbf(ga0 * rbf(z0)), x2v.y + rbf(ga1 * rbf(z1)));
        __builtin_amdgcn_raw_buffer_store_b32(o, mkrs(out, D * 2), moff(lane < 8, opr * 4), 0, 0);
        if (f == 0 && w == 5) stsc1(tag, rw, moff(lane == 0, WS_EP));
        STAMP(11);
      }
    }
  }
}

extern "C" void kernel_launch(void* const* d_in, const int* in_sizes, int n_in, void* d_out, int out_size, void* d_ws, size_t ws_size,
                              hipStream_t stream) {
  hipLaunchKernelGGL(mega, dim3(256), dim3(512), 0, stream, (const u16*)d_in[0], (const u16*)d_in[1], (const u16*)d_in[2], (const u16*)d_in[3],
                     (const u16*)d_in[4], (const u16*)d_in[6], (const u16*)d_in[7], (const u16*)d_in[8], (const u16*)d_in[9], (const u16*)d_in[10],
                     (u16*)d_out, (unsigned char*)d_ws);
}
```

```cpp
#include <hip/hip_runtime.h>
#include <hip/hip_bf16.h>
#include <cstdint>

typedef unsigned short u16;
typedef unsigned v4u __attribute__((ext_vector_type(4)));
typedef unsigned v2u __attribute__((ext_vector_type(2)));
typedef float v4f __attribute__((ext_vector_type(4)));
typedef float v2f __attribute__((ext_vector_type(2)));
typedef __bf16 bf16x2 __attribute__((ext_vector_type(2)));
typedef __amdgpu_buffer_rsrc_t rsrc_t;
#define LAS __attribute__((address_space(3)))
#define GAS __attribute__((address_space(1)))
#define DEV __device__ __forceinline__

#define CP 5
#define SP 2
#define SP2 5
#define LA 3
#define QP 5
#define TRG 2
#define SPV SP
#define KD 11
#define TLBW 0
#define ILV 1
#define MAP 0
#define FPW CP
#define HOLD 0
#define REP 1
#define EPW 2
#define WS1 3
#define TAU 76
#define TAUV TAU
#define TA0 -30
#define WQE 1
#define OAUX 0
#define BW 3
#define OLD11 1
#define PRIO 0
#define SEQ 4
#define MLP 1
#define AUXL 16
#define AUXS 16
#define EPS 0
#define PB 0
#define H4A 16
#define TMOD 1600
#define H4P 0
#define EPO 0
#define WY 0
#define PK 1
#define KI 2
#define MLT 9
#define SLD 0
#define SLM 2
#define SLO 1
#define SLW 0
#define PFKB 25
#define WOP 1

constexpr int D = 640, SK = 4096, DH = 192, FF = 1536;
constexpr int AUX_NT = 2, AUX_SC1 = 16;
constexpr int WS_EP = 0, WS_PROG = 2048, WS_MODG = 4096, WS_QG = 32768, WS_MLG = 65536, WS_OP = 131072, WS_WP = 2 << 20, WP_REP = 81920, WS_SAG = 98304, WS_DP = 786432, WS_BYTES = 3 << 20;
constexpr unsigned SPIN_CAP = 1u << 17;
constexpr float LOG2E = 1.4426950408889634f;
constexpr float QSCALE = 0.07216878364870322f;

#define STAMP(i) do { } while (0)

constexpr int LDS_RED = 0;
constexpr int LDS_VEC = 6144;
constexpr int LDS_VEC2 = 8704;
constexpr int LDS_HB = 11264;
constexpr int LDS_QS = 12800;
constexpr int LDS_STAMP = 15872;
constexpr int LDS_BIG = 16896;
constexpr int LDS_BYTES = 16896 + 81920;

DEV float lo16(unsigned w) { return __uint_as_float(w << 16); }
DEV float hi16(unsigned w) { return __uint_as_float(w & 0xffff0000u); }
DEV float f32(unsigned w) { return __uint_as_float(w); }
DEV unsigned u32(float f) { return __float_as_uint(f); }
DEV float rbf(float f) { return (float)(__bf16)f; }
DEV unsigned pack2(float a, float b) { bf16x2 v = {(__bf16)a, (__bf16)b}; return __builtin_bit_cast(unsigned, v); }
DEV float dot2(unsigned a, unsigned b, float acc) { return __builtin_amdgcn_fdot2_f32_bf16(__builtin_bit_cast(bf16x2, a), __builtin_bit_cast(bf16x2, b), acc, false); }
template <int CTRL> DEV float dpp(float x) { return __builtin_bit_cast(float, __builtin_amdgcn_mov_dpp(__builtin_bit_cast(int, x), CTRL, 0xf, 0xf, true)); }
constexpr int XOR1 = 0xB1, XOR2 = 0x4E, XOR7 = 0x141, XOR8 = 0x128, ROR4 = 0x124;
DEV float fold32(float a, float b) { auto s = __builtin_amdgcn_permlane32_swap(u32(a), u32(b), false, false); return f32(s[0]) + f32(s[1]); }
DEV float fold16(float a, float b) { auto s = __builtin_amdgcn_permlane16_swap(u32(a), u32(b), false, false); return f32(s[0]) + f32(s[1]); }
DEV float max32(float a) { auto s = __builtin_amdgcn_permlane32_swap(u32(a), u32(a), false, false); return fmaxf(f32(s[0]), f32(s[1])); }
DEV float max16(float a) { auto s = __builtin_amdgcn_permlane16_swap(u32(a), u32(a), false, false); return fmaxf(f32(s[0]), f32(s[1])); }
DEV float wave_sum(float x) { x += dpp<XOR1>(x); x += dpp<XOR2>(x); x += dpp<XOR7>(x); x += dpp<XOR8>(x); x = fold16(x, x); return fold32(x, x); }
DEV float wave_max(float x) { x = fmaxf(x, dpp<XOR1>(x)); x = fmaxf(x, dpp<XOR2>(x)); x = fmaxf(x, dpp<XOR7>(x)); x = fmaxf(x, dpp<XOR8>(x)); x = max16(x); return max32(x); }
DEV rsrc_t mkrs(const void* p, int bytes) {
  const unsigned long long a = (unsigned long long)p;
  const unsigned long long u = ((unsigned long long)(unsigned)__builtin_amdgcn_readfirstlane((int)(a >> 32)) << 32) | (unsigned)__builtin_amdgcn_readfirstlane((int)a);
  return __builtin_amdgcn_make_buffer_rsrc((void*)u, 0, __builtin_amdgcn_readfirstlane(bytes), 0x00020000);
}
DEV v4u ldnt(rsrc_t rs, int voff, int soff) { return __builtin_amdgcn_raw_buffer_load_b128(rs, voff, soff, AUX_NT); }
#define MEMCLOB() asm volatile("" ::: "memory")
DEV v4u ldsc4(rsrc_t rs, int voff, int soff = 0) { return __builtin_amdgcn_raw_buffer_load_b128(rs, voff, soff, AUXL); }
DEV v2u ldsc2(rsrc_t rs, int voff, int soff = 0) { return __builtin_amdgcn_raw_buffer_load_b64(rs, voff, soff, AUXL); }
DEV unsigned ldsc1(rsrc_t rs, int voff) { return __builtin_amdgcn_raw_buffer_load_b32(rs, voff, 0, AUXL); }
DEV void stsc4(v4u v, rsrc_t rs, int voff) { __builtin_amdgcn_raw_buffer_store_b128(v, rs, voff, 0, AUXS); }
DEV void stsc2(v2u v, rsrc_t rs, int voff) { __builtin_amdgcn_raw_buffer_store_b64(v, rs, voff, 0, AUXS); }
DEV void stsc1(unsigned v, rsrc_t rs, int voff) { __builtin_amdgcn_raw_buffer_store_b32(v, rs, voff, 0, AUXS); }
DEV int moff(bool on, int off) { return on ? off : 0x40000000; }
DEV unsigned pk24(float f, unsigned t8) { const unsigned u = u32(f); return ((u + 0x7fu + ((u >> 8) & 1u)) & 0xffffff00u) | t8; }
DEV unsigned bad4(v4u v, unsigned tag) { return (v.y ^ tag) | (v.w ^ tag); }
#define PIN24(a) asm volatile("" : "+v"(a[0]), "+v"(a[1]), "+v"(a[2]), "+v"(a[3]), "+v"(a[4]), "+v"(a[5]), "+v"(a[6]), "+v"(a[7]), "+v"(a[8]), "+v"(a[9]), "+v"(a[10]), "+v"(a[11]), \
  "+v"(a[12]), "+v"(a[13]), "+v"(a[14]), "+v"(a[15]), "+v"(a[16]), "+v"(a[17]), "+v"(a[18]), "+v"(a[19]), "+v"(a[20]), "+v"(a[21]), "+v"(a[22]), "+v"(a[23]))
template <int N> DEV void waitvm() { asm volatile("s_waitcnt vmcnt(%0)" ::"i"(N) : "memory"); }
#define SB() __builtin_amdgcn_sched_barrier(0)
DEV void lds_barrier() { asm volatile("s_waitcnt lgkmcnt(0)" ::: "memory"); __builtin_amdgcn_s_barrier(); asm volatile("" ::: "memory"); }
DEV void lds_fence() { asm volatile("s_waitcnt lgkmcnt(0)" ::: "memory"); }

DEV float reduce8_over32(const float (&a)[8], int lane) {
  float y[4], z[2];
#pragma unroll
  for (int i = 0; i < 4; ++i) y[i] = fold16(a[i], a[i + 4]);
#pragma unroll
  for (int i = 0; i < 2; ++i) { const float t0 = y[i] + dpp<XOR8>(y[i]), t1 = y[i + 2] + dpp<XOR8>(y[i + 2]); z[i] = (lane & 8) ? t1 : t0; }
  const float t0 = z[0] + dpp<XOR7>(z[0]), t1 = z[1] + dpp<XOR7>(z[1]);
  float v = (lane & 4) ? t1 : t0;
  v += dpp<XOR2>(v); v += dpp<XOR1>(v);
  return v;
}
DEV float sum8(float x) { x += dpp<XOR1>(x); x += dpp<XOR2>(x); x += dpp<XOR7>(x); return x; }
DEV void pairperm(v4u e, v4u o, unsigned (&p)[8]) {
#pragma unroll
  for (int k = 0; k < 4; ++k) { p[2 * k] = __builtin_amdgcn_perm(o[k], e[k], 0x05040100u); p[2 * k + 1] = __builtin_amdgcn_perm(o[k], e[k], 0x07060302u); }
  asm volatile("" : "+v"(p[0]), "+v"(p[1]), "+v"(p[2]), "+v"(p[3]), "+v"(p[4]), "+v"(p[5]), "+v"(p[6]), "+v"(p[7]));
}
DEV void fma8(float (&acc)[24], int base, float p, v4u v) {
#pragma unroll
  for (int k = 0; k < 4; ++k) { acc[base + 2 * k] = fmaf(p, lo16(v[k]), acc[base + 2 * k]); acc[base + 2 * k + 1] = fmaf(p, hi16(v[k]), acc[base + 2 * k + 1]); }
}
DEV void unit_prep(const v4u (&raw)[10], unsigned (&p)[5][8]) {
#pragma unroll
  for (int i = 0; i < 5; ++i) pairperm(raw[2 * i], raw[2 * i + 1], p[i]);
}
DEV void unit_partial(const unsigned (&p)[5][8], LAS unsigned char* L, int vecoff, int redoff, int w, int lane) {
  const LAS unsigned* sc = (const LAS unsigned*)(L + vecoff) + 40 * w + (lane >> 3);
  float a[8];
#pragma unroll
  for (int k = 0; k < 8; ++k) a[k] = 0.f;
#pragma unroll
  for (int i = 0; i < 5; ++i) { const unsigned hv = sc[8 * i];
#pragma unroll
    for (int k = 0; k < 8; ++k) a[k] = dot2(p[i][k], hv, a[k]); }
  float y[4], z[2];
#pragma unroll
  for (int i = 0; i < 4; ++i) y[i] = fold32(a[i], a[i + 4]);
#pragma unroll
  for (int i = 0; i < 2; ++i) z[i] = fold16(y[i], y[i + 2]);
  const float t0 = z[0] + dpp<XOR8>(z[0]), t1 = z[1] + dpp<XOR8>(z[1]);
  ((LAS float*)(L + redoff))[w * 64 + (lane & 7) * 8 + (lane >> 3)] = (lane & 8) ? t1 : t0;
}
DEV v4u ldp(rsrc_t rs, int voff, int soff) { waitvm<CP>(); const v4u v = ldnt(rs, voff, soff); SB(); return v; }
DEV void unit_publish(LAS unsigned char* L, int redoff, int lane, int outoff, unsigned tag, rsrc_t rw) {
  const LAS v2f* red = (const LAS v2f*)(L + redoff) + (lane & 31);
  v2f s = red[0];
#pragma unroll
  for (int i = 1; i < 8; ++i) s += red[i * 32];
  stsc2(v2u{pack2(s.x, s.y), tag}, rw, moff(lane < 32, outoff + lane * 8));
}
template <int LO, int HI, bool PACED> DEV void unit_load(v4u (&raw)[10], const u16* wmod, const u16* wq, bool is_mod, int g, bool on, int w, int lane) {
  const int stride = is_mod ? 6 * D * 2 : 32 * DH * 2;
  const rsrc_t rs = mkrs((const unsigned char*)(is_mod ? wmod : wq) + (size_t)80 * w * stride, on ? 80 * stride : 0);
  const int vo = (lane >> 3) * 2 * stride + g * 128 + (lane & 7) * 16;
#pragma unroll
  for (int i = LO; i < HI; ++i) raw[i] = (PACED ? ldp(rs, vo, (16 * (i >> 1) + (i & 1)) * stride) : ldnt(rs, vo, (16 * (i >> 1) + (i & 1)) * stride));
  SB();
}

__global__ void __launch_bounds__(512, 2)
mega(const u16* x, const u16* Kc, const u16* Vc, const u16* wq, const u16* wo,
     const u16* wg, const u16* wu, const u16* wd, const u16* cvec, const u16* wmod,
     u16* out, unsigned char* ws) {
  __shared__ __attribute__((aligned(16))) unsigned char smem[LDS_BYTES];
  LAS unsigned char* L = (LAS unsigned char*)smem;
  const int px = blockIdx.x & 7, pk = blockIdx.x >> 3;
  const int tid = threadIdx.x, lane = tid & 63, w = __builtin_amdgcn_readfirstlane(tid >> 6);
  const int r = lane >> 3, c = lane & 7;
  v4u pf[4];
  if (pk < 8) {
    const rsrc_t rc = mkrs((const void*)(__builtin_amdgcn_s_getpc() & ~127ull), PFKB * 1024);
#pragma unroll
    for (int i = 0; i < 4; ++i) pf[i] = __builtin_amdgcn_raw_buffer_load_b128(rc, tid * 16, i * 8192, 0);
    MEMCLOB();
  }
  const int ph_ = pk >= 20 ? 1 : 0, P = 2 * px + ph_, cg = pk - 8 - 12 * ph_, hq = cg >= 6 ? 1 : 0, s = cg - 6 * hq, h = 2 * P + hq;
  const int rem = s < 2 ? 6 : 5, t0cu = 85 * s + (s < 2 ? s : 2);
  const int wt = OLD11 ? w : 7 - w;
  const int nt = wt < rem ? 11 : 10, TS = 8 * 3072;
  int nrec = (80 + rem - wt) * 3072;
  unsigned kvoff = (unsigned)(h * 512 + t0cu + wt) * 3072u;
  asm volatile("" : "+s"(nrec), "+s"(kvoff));
  asm volatile("" ::"s"(x), "s"(Kc), "s"(Vc), "s"(wq), "s"(wo), "s"(wg), "s"(wu), "s"(wd), "s"(cvec), "s"(wmod), "s"(out), "s"(ws));
  const rsrc_t rw = mkrs(ws, WS_BYTES);
  if (__builtin_expect(pk >= 8, 1)) {
    if (PRIO && w >= 4) __builtin_amdgcn_s_setprio(1);
    const rsrc_t rk = mkrs((const unsigned char*)Kc + kvoff, nrec), rv = mkrs((const unsigned char*)Vc + kvoff, nrec);
    const rsrc_t rwo = mkrs(wo + (size_t)(384 * P + 48 * w) * D + 64 * cg, (cg < 10 && (!WY || w < WY)) ? 48 * D * 2 - 128 * cg : 0);
    const int voff = r * 384 + c * 16, l48 = lane < 48 ? lane : 47;
    v4u kr[11][3], vr[12][3];
    unsigned tag;
    const unsigned tst = TAU ? (unsigned)__builtin_amdgcn_s_memrealtime() + TA0 : 0u;
#define PACE(tt, lo, hi) do { if (TAU) { waitvm<hi>(); if ((int)((unsigned)__builtin_amdgcn_s_memrealtime() - tst - (tt)) < 0) waitvm<lo>(); } else waitvm<lo>(); } while (0)
#define LDV(t) do { PACE(10 * TAU + 2 * TAU / 3 + (t) * TAUV, SPV, SPV + BW); _Pragma("unroll") for (int j = 0; j < 3; ++j) vr[t][j] = ldnt(rv, voff + j * 128, (t) * TS); SB(); } while (0)
#define LDVN(t) do { PACE(10 * TAU + 2 * TAU / 3 + (t) * TAUV, SP2, SP2 + BW); _Pragma("unroll") for (int j = 0; j < 3; ++j) vr[t][j] = ldnt(rv, voff + j * 128, (t) * TS); SB(); } while (0)
#define VPERM(u) do { _Pragma("unroll") for (int j = 0; j < 3; ++j) pairperm(vr[2 * (u)][j], vr[2 * (u) + 1][j], vp[u][j]); SB(); } while (0)
#define PV(u) do { _Pragma("unroll") for (int j = 0; j < 3; ++j) _Pragma("unroll") for (int k = 0; k < 8; ++k) acc[j * 8 + k] = dot2(vp[u][j][k], pp[u], acc[j * 8 + k]); PIN24(acc); SB(); } while (0)
#pragma unroll
    for (int t = 0; t < 11; ++t) {
      if (t >= KI) PACE(t * TAU, SP, SP + BW);
      if (t == 3) STAMP(9);
      if (t == 1) STAMP(10);
      if (t == 2) STAMP(11);
      if (t == 8) tag = EPS ? *(const __attribute__((address_space(4))) unsigned*)(ws + WS_EP) : ldsc1(rw, WS_EP);
#pragma unroll
      for (int j = 0; j < 3; ++j) { kr[t][j] = ldnt(rk, voff + j * 128, t * TS); SB(); }
    }
    unsigned vp[6][3][8];
    v4u qg;
    static_assert(QP >= 1 && QP <= 5, "");
#pragma unroll
    for (int t = 0; t <= QP + 1; ++t) {
      if (t == QP) { qg = ldsc4(rw, WS_QG + h * 768 + l48 * 16); SB(); }
      LDV(t);
      if (t == 0) { tag += 1u; asm volatile("" : "+v"(tag)); SB(); }
      if (t == 3) VPERM(0);
      if (t == 5) VPERM(1);
    }
    STAMP(1);
    if (__any(bad4(qg, tag) != 0u)) {
#pragma unroll 1
      for (unsigned sp = 0; sp < SPIN_CAP; ++sp) {
        MEMCLOB();
        qg = ldsc4(rw, WS_QG + h * 768 + l48 * 16);
        if (!__any(bad4(qg, tag) != 0u)) break;
        __builtin_amdgcn_s_sleep(2);
      }
    }
    STAMP(2);
    LAS unsigned* qs = (LAS unsigned*)(L + LDS_QS) + w * 96;
    *(LAS v2u*)(qs + 2 * l48) = v2u{qg.x, qg.z};
    lds_fence();
    v4u qp[3];
#pragma unroll
    for (int j = 0; j < 3; ++j) qp[j] = *(const LAS v4u*)(qs + j * 32 + c * 4);
    float sc[12];
#pragma unroll
    for (int t = 0; t < 11; ++t) {
      float d = 0.f;
#pragma unroll
      for (int j = 0; j < 3; ++j)
#pragma unroll
        for (int k = 0; k < 4; ++k) d = dot2(kr[t][j][k], qp[j][k], d);
      d += dpp<XOR1>(d); d += dpp<XOR2>(d); d += dpp<XOR7>(d);
      d = rbf(rbf(d) * QSCALE);
      sc[t] = (t < 10 || nt == 11) ? d : -INFINITY;
      asm volatile("" : "+v"(sc[t]));
      if (t == 4) { SB(); LDVN(QP + 2); }
      if (t == 10) { SB(); LDVN(QP + 3); }
    }
    float mw = sc[0];
#pragma unroll
    for (int t = 1; t < 11; ++t) mw = fmaxf(mw, sc[t]);
    mw = fmaxf(mw, dpp<XOR8>(mw)); mw = max16(mw); mw = max32(mw);
    float lw = 0.f;
#pragma unroll
    for (int t = 0; t < 11; ++t) { sc[t] = __builtin_amdgcn_exp2f((sc[t] - mw) * LOG2E); lw += sc[t]; }
    sc[11] = 0.f;
    lw += dpp<XOR8>(lw); lw = fold16(lw, lw); lw = fold32(lw, lw);
    unsigned pp[6];
#pragma unroll
    for (int u = 0; u < 6; ++u) pp[u] = pack2(sc[2 * u], sc[2 * u + 1]);
    ((LAS v2f*)(L + LDS_HB))[w] = v2f{mw, lw};
    float acc[24];
#pragma unroll
    for (int i = 0; i < 24; ++i) acc[i] = 0.f;
    v4u wr[6];
#define LDWO() LDWR(0, 6)
#define LDWR(lo, hi) do { _Pragma("unroll") for (int i = lo; i < hi; ++i) wr[i] = ldnt(rwo, r * (2 * D * 2) + c * 16, (16 * (i >> 1) + (i & 1)) * D * 2); SB(); } while (0)
    float fw;
#define MLPUB() do { \
      lds_barrier(); \
      STAMP(3); \
      const v2f ml = ((const LAS v2f*)(L + LDS_HB))[c]; \
      float M = ml.x; M = fmaxf(M, dpp<XOR1>(M)); M = fmaxf(M, dpp<XOR2>(M)); M = fmaxf(M, dpp<XOR7>(M)); \
      const float Lc = sum8(ml.y * __builtin_amdgcn_exp2f((ml.x - M) * LOG2E)); \
      fw = __builtin_amdgcn_exp2f((mw - M) * LOG2E); \
      asm volatile("" : "+v"(fw)); \
      if (w == 7) stsc4(v4u{u32(M), tag, u32(Lc), tag}, rw, moff(lane < 8, WS_MLG + (h * 8 + s) * 128 + lane * 16)); \
      SB(); } while (0)
    static_assert(QP == 5, "sequence below");
    const int hh = 2 * P + (w >> 2), mloff = WS_MLG + (hh * 8 + (c < 6 ? c : 5)) * 128;
    MLPUB(); LDVN(9); PV(0); PV(1); VPERM(2); PV(2); LDVN(10); VPERM(3); PV(3);
    VPERM(4); PV(4);
    v4u ml;
    if (MLP == 0) ml = ldsc4(rw, mloff);
    SB();
    LDWO();
#pragma unroll
    for (int j = 0; j < 3; ++j) fma8(acc, j * 8, lo16(pp[5]), vr[10][j]);
    PIN24(acc); SB();
    STAMP(4);
    {
      float y[12], z[6];
#pragma unroll
      for (int i = 0; i < 12; ++i) y[i] = fold32(acc[i], acc[i + 12]);
#pragma unroll
      for (int i = 0; i < 6; ++i) z[i] = fold16(y[i], y[i + 6]);
      LAS float* red = (LAS float*)(L + LDS_RED) + w * 192 + c * 24 + r * 3;
#pragma unroll
      for (int i = 0; i < 3; ++i) { const float t0 = z[i] + dpp<XOR8>(z[i]), t1 = z[i + 3] + dpp<XOR8>(z[i + 3]); red[i] = ((lane & 8) ? t1 : t0) * fw; }
    }
    if (MLP == 1) ml = ldsc4(rw, mloff);
    lds_barrier();
    if (w < 2) {
      const int dp = 48 * w + l48;
      const LAS v2f* red = (const LAS v2f*)(L + LDS_RED) + ((dp >> 2) & 7) * 12 + (dp >> 5) * 4 + (dp & 3);
      v2f t[8];
#pragma unroll
      for (int i = 0; i < 8; ++i) t[i] = red[i * 96];
      const v2f o = ((t[0] + t[1]) + (t[2] + t[3])) + ((t[4] + t[5]) + (t[6] + t[7]));
      stsc4(v4u{u32(o.x), tag, u32(o.y), tag}, rw, moff(lane < 48, WS_OP + (h * 8 + s) * 2048 + dp * 16));
    }
    SB();
    if (WY && w >= WY) {
      const rsrc_t rwo2 = mkrs(wo + (size_t)(384 * P + 48 * w) * D + 64 * cg, cg < 10 ? 48 * D * 2 - 128 * cg : 0);
#pragma unroll
      for (int i = 0; i < 6; ++i) wr[i] = ldnt(rwo2, r * (2 * D * 2) + c * 16, (16 * (i >> 1) + (i & 1)) * D * 2);
      SB();
    }
    STAMP(5);
    if (cg < 10) {
      const int opoff = WS_OP + (hh * 8 + (c < 6 ? c : 5)) * 2048 + (24 * (w & 3) + r) * 16;
      v4u op[3];
      if (EPO) {
        waitvm<EPO>();
#pragma unroll
        for (int i = 0; i < 3; ++i) op[i] = ldsc4(rw, opoff + i * 128);
        SB();
      }
      unsigned wp[3][8];
      if (__any(bad4(ml, tag) != 0u)) {
#pragma unroll 1
        for (unsigned sp = 0; sp < SPIN_CAP; ++sp) {
          MEMCLOB();
          ml = ldsc4(rw, mloff);
          if (!__any(bad4(ml, tag) != 0u)) break;
          __builtin_amdgcn_s_sleep(SLM);
        }
      }
      float wv;
      {
        float M = f32(ml.x); M = fmaxf(M, dpp<XOR1>(M)); M = fmaxf(M, dpp<XOR2>(M)); M = fmaxf(M, dpp<XOR7>(M));
        const float e = c < 6 ? __builtin_amdgcn_exp2f((f32(ml.x) - M) * LOG2E) : 0.f;
        wv = e / sum8(e * f32(ml.z));
        asm volatile("" : "+v"(wv));
      }
      if (!EPO) {
#pragma unroll
        for (int i = 0; i < 3; ++i) op[i] = ldsc4(rw, opoff + i * 128);
        SB();
      }
#pragma unroll
      for (int i = 0; i < 3; ++i) pairperm(wr[2 * i], wr[2 * i + 1], wp[i]);
      STAMP(6);
      if (__any((bad4(op[0], tag) | bad4(op[1], tag) | bad4(op[2], tag)) != 0u)) {
#pragma unroll 1
        for (unsigned sp = 0; sp < SPIN_CAP; ++sp) {
          MEMCLOB();
#pragma unroll
          for (int i = 0; i < 3; ++i) op[i] = ldsc4(rw, opoff + i * 128);
          if (!__any((bad4(op[0], tag) | bad4(op[1], tag) | bad4(op[2], tag)) != 0u)) break;
          __builtin_amdgcn_s_sleep(SLO);
        }
      }
      STAMP(7);
      unsigned ov[3];
#pragma unroll
      for (int i = 0; i < 3; ++i) ov[i] = pack2(sum8(wv * f32(op[i].x)), sum8(wv * f32(op[i].z)));
      float a[8];
#pragma unroll
      for (int k = 0; k < 8; ++k) a[k] = dot2(wp[2][k], ov[2], dot2(wp[1][k], ov[1], dot2(wp[0][k], ov[0], 0.f)));
      float y[4], z[2];
#pragma unroll
      for (int i = 0; i < 4; ++i) y[i] = fold32(a[i], a[i + 4]);
#pragma unroll
      for (int i = 0; i < 2; ++i) { z[i] = fold16(y[i], y[i + 2]); z[i] += dpp<XOR8>(z[i]); }
      ((LAS float*)(L + LDS_VEC))[w * 64 + c * 8 + r] = (lane & 8) ? z[1] : z[0];
      lds_barrier();
      if (w == 2) {
        const LAS v4f* red = (const LAS v4f*)(L + LDS_VEC) + (lane & 15);
        v4f t[8];
#pragma unroll
        for (int i = 0; i < 8; ++i) t[i] = red[i * 16];
        const v4f o = ((t[0] + t[1]) + (t[2] + t[3])) + ((t[4] + t[5]) + (t[6] + t[7]));
        const unsigned ta = tag & 0xffu, tb = (tag >> 8) & 0xffu;
        const v4u ov4 = {pk24(o.x, ta), pk24(o.y, tb), pk24(o.z, ta), pk24(o.w, tb)};
#pragma unroll
        for (int q = 0; q < (REP ? 2 : 1); ++q)
          __builtin_amdgcn_raw_buffer_store_b128(ov4, rw, moff(REP || lane < 16, WS_WP + (lane >> 4) * WP_REP + (P * D + 64 * cg + 4 * (lane & 15)) * 4), q * 4 * WP_REP, AUX_SC1);
      }
      STAMP(8);
    }
  } else {
    const int b = blockIdx.x, xg = px, v = pk;
    const int t320 = tid < 320 ? tid : 319;
    unsigned tag, cpair;
#define LDC() do { MEMCLOB(); cpair = ((const GAS unsigned*)cvec)[t320]; tag = ldsc1(rw, WS_EP); SB(); } while (0)
    unsigned xpair, xall[5];
    float ln0, ln1;
    v4u u0[10], u1[10], sg[4];
    unsigned p0[5][8], p1[5][8];
    const int rp = lane & 31, hf = lane >> 5;
#define FASTG(p) ((p) + ((p) + 4) / 7)
#define LDX() do { xpair = ((const GAS unsigned*)x)[t320]; _Pragma("unroll") for (int i = 0; i < 5; ++i) xall[i] = ((const GAS unsigned*)x)[lane + 64 * i]; SB(); } while (0)
#define SILU() do { asm volatile("" ::"v"(pf[0]), "v"(pf[1]), "v"(pf[2]), "v"(pf[3])); tag += 1u; const float c0 = lo16(cpair), c1 = hi16(cpair); \
      ((LAS unsigned*)(L + LDS_VEC))[t320] = pack2(c0 / (1.f + expf(-c0)), c1 / (1.f + expf(-c1))); SB(); } while (0)
#define LNX() do { float sm = 0.f; \
      _Pragma("unroll") for (int i = 0; i < 5; ++i) sm += lo16(xall[i]) + hi16(xall[i]); \
      const float mean = wave_sum(sm) * (1.f / D); float sq = 0.f; \
      _Pragma("unroll") for (int i = 0; i < 5; ++i) { const float a = lo16(xall[i]) - mean, bb = hi16(xall[i]) - mean; sq += a * a + bb * bb; } \
      const float var = wave_sum(sq) * (1.f / D); \
      const float mb = rbf(mean), sd = rbf(sqrtf(rbf(rbf(var) + 1e-5f))); \
      ln0 = rbf(rbf(lo16(xpair) - mb) / sd); ln1 = rbf(rbf(hi16(xpair) - mb) / sd); \
      asm volatile("" : "+v"(ln0), "+v"(ln1)); SB(); } while (0)
#define LDSAG() do { _Pragma("unroll") for (int i = 0; i < 4; ++i) sg[i] = ldsc4(rw, moff(tid < 320, WS_SAG + tid * 16), (i & 1) * 10240 + (i >> 1) * 5120); } while (0)
#define MAKEH() do { if (w < 5) { \
      if (__any((bad4(sg[0], tag) | bad4(sg[1], tag) | bad4(sg[2], tag) | bad4(sg[3], tag)) != 0u)) { \
        _Pragma("unroll 1") for (unsigned sp = 0; sp < SPIN_CAP; ++sp) { \
          MEMCLOB(); LDSAG(); \
          if (!__any((bad4(sg[0], tag) | bad4(sg[1], tag) | bad4(sg[2], tag) | bad4(sg[3], tag)) != 0u)) break; \
          __builtin_amdgcn_s_sleep(1); } } \
      const float sa0 = rbf(f32(sg[0].x) + f32(sg[1].x)), sa1 = rbf(f32(sg[0].z) + f32(sg[1].z)); \
      const float ca0 = rbf(f32(sg[2].x) + f32(sg[3].x)), ca1 = rbf(f32(sg[2].z) + f32(sg[3].z)); \
      ((LAS unsigned*)(L + LDS_VEC2))[tid] = pack2(rbf(ln0 * rbf(1.f + ca0)) + sa0, rbf(ln1 * rbf(1.f + ca1)) + sa1); } \
      STAMP(2); lds_barrier(); } while (0)
    if (b < 40) {
      const int wi = 5 * xg + v, ga_ = wi < 20 ? wi : wi - 20, ha_ = wi < 20 ? 0 : 1, uu = b - 8;
      const bool two = v == 0 || (uu >= 12 && uu < 18);
      const int g0 = v == 0 ? FASTG(20 + xg) : (uu < 12 ? 8 * uu + 3 : FASTG(uu - 12));
      const int g1 = v == 0 ? FASTG(28 + xg) : FASTG(24 + uu);
      const unsigned tw0 = (unsigned)__builtin_amdgcn_s_memrealtime();
      v4u ua[6];
      LDC();
      LDX();
      {
        const rsrc_t ra = mkrs(wmod + (size_t)(320 * ha_ + 40 * w) * (6 * D), 40 * 6 * D * 2);
#pragma unroll
        for (int i = 0; i < 6; ++i) ua[i] = ldnt(ra, 2 * r * (6 * D * 2) + ga_ * 128 + c * 16, (16 * (i >> 1) + (i & 1)) * (6 * D * 2));
        SB();
      }
      SILU();
      lds_barrier();
      unsigned hv[3];
      {
        const LAS unsigned* sc = (const LAS unsigned*)(L + LDS_VEC) + 160 * ha_ + 20 * w + r;
        hv[0] = sc[0]; hv[1] = sc[8]; hv[2] = sc[16]; hv[2] = r < 4 ? hv[2] : 0u;
      }
      STAMP(8);
      unit_load<0, 5, true>(u0, wmod, wq, false, g0, true, w, lane);
      STAMP(9);
      {
        unsigned pa[3][8];
#pragma unroll
        for (int i = 0; i < 3; ++i) pairperm(ua[2 * i], ua[2 * i + 1], pa[i]);
        float a[8];
#pragma unroll
        for (int k = 0; k < 8; ++k) a[k] = dot2(pa[2][k], hv[2], dot2(pa[1][k], hv[1], dot2(pa[0][k], hv[0], 0.f)));
        float y[4], z[2];
#pragma unroll
        for (int i = 0; i < 4; ++i) y[i] = fold32(a[i], a[i + 4]);
#pragma unroll
        for (int i = 0; i < 2; ++i) z[i] = fold16(y[i], y[i + 2]);
        const float t0 = z[0] + dpp<XOR8>(z[0]), t1 = z[1] + dpp<XOR8>(z[1]);
        ((LAS float*)(L + LDS_RED))[w * 64 + c * 8 + r] = (lane & 8) ? t1 : t0;
        lds_barrier();
        if (w == 0) {
          const LAS v2f* red = (const LAS v2f*)(L + LDS_RED) + (lane & 31);
          v2f sm = red[0];
#pragma unroll
          for (int i = 1; i < 8; ++i) sm += red[i * 32];
          stsc4(v4u{u32(sm.x), tag, u32(sm.y), tag}, rw, moff(lane < 32, WS_SAG + ha_ * 10240 + ga_ * 512 + lane * 16));
        }
      }
      STAMP(1);
      SB();
      unit_load<5, 10, true>(u0, wmod, wq, false, g0, true, w, lane);
      LNX();
      if (two) unit_load<0, 10, true>(u1, wmod, wq, false, g1, true, w, lane);
      waitvm<2>();
      LDSAG();
      SB();
      unit_prep(u0, p0);
      if (two) unit_prep(u1, p1);
      SB();
      MAKEH();
      unit_partial(p0, L, LDS_VEC2, LDS_RED + 2048, w, lane);
      if (two) unit_partial(p1, L, LDS_VEC2, LDS_RED + 4096, w, lane);
      lds_barrier();
      if (w == 0 || (w == 1 && two)) unit_publish(L, LDS_RED + 2048 + w * 2048, lane, WS_QG + (w ? g1 : g0) * 256, tag, rw);
      STAMP(13);
      SB();
      while ((int)((unsigned)__builtin_amdgcn_s_memrealtime() - tw0 - TMOD) < 0) __builtin_amdgcn_s_sleep(8);
      unit_load<0, 10, false>(u0, wmod, wq, true, 20 + b, true, w, lane);
      unit_prep(u0, p0);
      unit_partial(p0, L, LDS_VEC, LDS_RED, w, lane);
      lds_barrier();
      if (w == 0) unit_publish(L, LDS_RED, lane, WS_MODG + (20 + b) * 256, tag, rw);
      STAMP(3);
    } else {
      const int f = 3 * px + pk - 5, fi = f - ((f + 4) >> 3);
      const int g0 = FASTG(42 + fi), g1 = FASTG(63 + fi);
      LDC();
      LDX();
      if ((f & 7) != 3) {
        unit_load<0, 6, false>(u0, wmod, wq, false, g0, true, w, lane);
        asm volatile("" : "+v"(tag), "+v"(xpair) : "v"(pf[0]), "v"(pf[1]), "v"(pf[2]), "v"(pf[3]), "v"(cpair));
        tag += 1u;
        unit_load<6, 10, true>(u0, wmod, wq, false, g0, true, w, lane);
        LNX();
        STAMP(1);
        unit_load<0, 8, true>(u1, wmod, wq, false, g1, true, w, lane);
        unit_prep(u0, p0);
        unit_load<8, 10, true>(u1, wmod, wq, false, g1, true, w, lane);
        waitvm<2>();
        LDSAG();
        SB();
        unit_prep(u1, p1);
        SB();
        MAKEH();
        unit_partial(p0, L, LDS_VEC2, LDS_RED + 2048, w, lane);
        unit_partial(p1, L, LDS_VEC2, LDS_RED + 4096, w, lane);
        lds_barrier();
        if (w < 2) unit_publish(L, LDS_RED + 2048 + w * 2048, lane, WS_QG + (w ? g1 : g0) * 256, tag, rw);
        STAMP(3);
        SB();
      } else {
        asm volatile("; no front work" : "+v"(tag), "+v"(xpair) : "v"(pf[0]), "v"(pf[1]), "v"(pf[2]), "v"(pf[3]), "v"(cpair));
        tag += 1u;
      }
      STAMP(5);
      SB();
      const rsrc_t rd = mkrs(wd + (size_t)f * 64 * D + w * 128, w < 5 ? 64 * D * 2 - w * 256 : 0);
      const rsrc_t rg = mkrs(wg + f * 64, D * FF * 2 - f * 128), ru = mkrs(wu + f * 64, D * FF * 2 - f * 128);
      const int gvo = (40 * w + r) * (FF * 2) + c * 16;
      v4u dr[16];
      unsigned gu[10][8], dw[8][8];
#define LDD(lo, hi) do { _Pragma("unroll") for (int i = lo; i < hi; ++i) { waitvm<FPW + 4>(); dr[i] = ldnt(rd, (lane >> 4) * (16 * D * 2) + (lane & 15) * 16, i * D * 2); SB(); } } while (0)
#define LDGU(nn, p, lo, hi) do { _Pragma("unroll") for (int i = lo; i < hi; ++i) { waitvm<FPW>(); nn[i] = ldnt(i < 5 ? rg : ru, gvo, (320 * (p) + 8 * (i % 5)) * FF * 2); SB(); } } while (0)
#define GUROUND(p, nn) do { \
        _Pragma("unroll") for (int i = 0; i < 5; ++i) { \
          const int row = 40 * w + 8 * i + r, a = row * 128 + ((c ^ ((row / 10) & 7)) << 4); \
          *(LAS v4u*)(L + LDS_BIG + a) = nn[i]; *(LAS v4u*)(L + LDS_BIG + 40960 + a) = nn[5 + i]; } \
        lds_barrier(); \
        _Pragma("unroll") for (int i = 0; i < 5; ++i) { \
          const LAS unsigned char* q = L + LDS_BIG + hf * 40960 + (10 * rp + 2 * i) * 128 + ((w ^ (rp & 7)) << 4); \
          pairperm(*(const LAS v4u*)q, *(const LAS v4u*)(q + 128), gu[(p) * 5 + i]); } \
        SB(); } while (0)
      LDGU(u0, 0, 0, 10);
      LDGU(u1, 1, 0, 6);
      GUROUND(0, u0);
      LDGU(u1, 1, 6, 10);
      LDD(0, 8);
      lds_barrier();
      GUROUND(1, u1);
      LDD(8, 16);
      const bool red_on = (w == 5 || w == 6) && f < 20;
      const int opr = 16 * f + 8 * (w - 5) + c;
#pragma unroll
      for (int i = 0; i < 8; ++i) pairperm(dr[2 * i], dr[2 * i + 1], dw[i]);
      STAMP(4);
      LAS v2f* stat = (LAS v2f*)(L + LDS_QS + 1024);
      float x20 = 0.f, x21 = 0.f;
      v2u ma, mb2, mc;
      const int pi = PK ? (tid & ~63) + 2 * rp + hf : tid;
      if (PK) xpair = (unsigned)__builtin_amdgcn_ds_bpermute((2 * rp + hf) * 4, (int)xpair);
      if (w >= 5) {
        stat[w] = v2f{0.f, 0.f};
        if (red_on) {
          if (H4P) {
            v4u dv[3];
#pragma unroll
            for (int i = 0; i < 3; ++i) dv[i] = ldsc4(rw, WS_DP + r * (D * 8) + opr * 16, i * 8 * D * 8);
            asm volatile("" ::"v"(dv[0]), "v"(dv[1]), "v"(dv[2]));
          }
#pragma unroll 1
          for (unsigned sp = 0; sp < SPIN_CAP; ++sp) {
            MEMCLOB();
            ma = ldsc2(rw, WS_MODG + (1600 + opr) * 8);
            if (!__any(ma.y != tag)) break;
            __builtin_amdgcn_s_sleep(4);
          }
        }
      } else {
        v4u pv[8];
        unsigned miss = 0x1ffu;
        const int wpoff = WS_WP + (REP ? px * WP_REP : 0) + (hf * D + 128 * w + 4 * rp) * 4;
        const unsigned ta = tag & 0xffu, tb = (tag >> 8) & 0xffu;
#pragma unroll 1
        for (unsigned sp = 0; sp < SPIN_CAP && miss; ++sp) {
          MEMCLOB();
#pragma unroll
          for (int g = 0; g < 8; ++g) if (__builtin_expect(miss & (1u << g), 0)) pv[g] = ldsc4(rw, wpoff, (2 * g) * D * 4);
          if (__builtin_expect(miss & 256u, 0)) { ma = ldsc2(rw, WS_MODG + (640 + pi) * 8); mb2 = ldsc2(rw, WS_MODG + (960 + pi) * 8); mc = ldsc2(rw, WS_MODG + (1280 + pi) * 8); }
#pragma unroll
          for (int g = 0; g < 8; ++g) if (__builtin_expect(miss & (1u << g), 0)) {
            const v4u e = pv[g] ^ v4u{ta, tb, ta, tb};
            if (!__any(((e.x | e.y | e.z | e.w) & 0xffu) != 0u)) {
              miss &= ~(1u << g);
              pv[g].x = u32(fold32(f32(e.x), f32(e.z))); pv[g].y = u32(fold32(f32(e.y), f32(e.w)));
            }
          }
          if (__builtin_expect(miss & 256u, 0)) {
            if (!__any(((ma.y ^ tag) | (mb2.y ^ tag) | (mc.y ^ tag)) != 0u)) { miss &= 255u; mc.x = pack2(1.f + lo16(mc.x), 1.f + hi16(mc.x)); }
          }
        }
        STAMP(6);
        const v2f at = {((f32(pv[0].x) + f32(pv[1].x)) + (f32(pv[2].x) + f32(pv[3].x))) + ((f32(pv[4].x) + f32(pv[5].x)) + (f32(pv[6].x) + f32(pv[7].x))),
                        ((f32(pv[0].y) + f32(pv[1].y)) + (f32(pv[2].y) + f32(pv[3].y))) + ((f32(pv[4].y) + f32(pv[5].y)) + (f32(pv[6].y) + f32(pv[7].y)))};
        x20 = rbf(lo16(xpair) + rbf(lo16(ma.x) * rbf(at.x))); x21 = rbf(hi16(xpair) + rbf(hi16(ma.x) * rbf(at.y)));
        *(LAS v2f*)(L + LDS_VEC2 + pi * 8) = v2f{x20, x21};
        const float piv = __builtin_bit_cast(float, __builtin_amdgcn_readfirstlane(__builtin_bit_cast(int, x20)));
        const float d0 = x20 - piv, d1 = x21 - piv;
        const float s1 = wave_sum(d0 + d1), s2 = wave_sum(d0 * d0 + d1 * d1);
        stat[w] = v2f{piv + s1 * (1.f / 128), s2 - s1 * s1 * (1.f / 128)};
      }
      lds_barrier();
      if (w < 5) {
        const v2f st = stat[c];
        const float mean = sum8(st.x) * 0.2f, dm = st.x - mean;
        const float m2 = sum8(c < 5 ? fmaf(128.f * dm, dm, st.y) : 0.f);
        const float mb = rbf(mean), sd = rbf(__builtin_amdgcn_sqrtf(rbf(rbf(fmaxf(m2, 0.f) * (1.f / D)) + 1e-5f)));
        const float rs = __builtin_amdgcn_rcpf(sd);
        const float l0 = rbf(rbf(x20 - mb) * rs), l1 = rbf(rbf(x21 - mb) * rs);
        ((LAS unsigned*)(L + LDS_HB))[pi] = pack2(rbf(l0 * lo16(mc.x)) + lo16(mb2.x), rbf(l1 * hi16(mc.x)) + hi16(mb2.x));
      }
      lds_barrier();
      STAMP(7);
      {
        const LAS unsigned* hb = (const LAS unsigned*)(L + LDS_HB) + 5 * rp;
        unsigned hv[10];
#pragma unroll
        for (int i = 0; i < 10; ++i) hv[i] = hb[(i / 5) * 160 + (i % 5)];
        float a[8];
#pragma unroll
        for (int k = 0; k < 8; ++k) a[k] = 0.f;
#pragma unroll
        for (int i = 0; i < 10; ++i)
#pragma unroll
          for (int k = 0; k < 8; ++k) a[k] = dot2(gu[i][k], hv[i], a[k]);
        const float vv = rbf(reduce8_over32(a, lane));
        auto sw = __builtin_amdgcn_permlane32_swap(u32(vv), u32(vv), false, false);
        ((LAS __bf16*)(L + LDS_QS))[8 * w + ((lane >> 2) & 7)] = (__bf16)(f32(sw[0]) * f32(sw[1]));
      }
      lds_barrier();
      STAMP(8);
      if (w < 5) {
        const LAS v4u* hp2 = (const LAS v4u*)(L + LDS_QS + (lane >> 4) * 32);
        const v4u h0 = hp2[0], h1 = hp2[1];
        float a[8];
#pragma unroll
        for (int k = 0; k < 8; ++k) a[k] = 0.f;
#pragma unroll
        for (int i = 0; i < 8; ++i) { const unsigned hv = i < 4 ? h0[i & 3] : h1[i & 3];
#pragma unroll
          for (int k = 0; k < 8; ++k) a[k] = dot2(dw[i][k], hv, a[k]); }
        float y[4];
#pragma unroll
        for (int i = 0; i < 4; ++i) y[i] = fold32(a[i], a[i + 4]);
        const float z0 = fold16(y[0], y[2]), z1 = fold16(y[1], y[3]);
        stsc4(v4u{u32(z0), tag, u32(z1), tag}, rw, WS_DP + (f * D + 128 * w + 8 * (lane & 15) + 4 * hf + 2 * ((lane >> 4) & 1)) * 8);
        STAMP(9);
      } else if (red_on) {
        const v2f x2v = ((const LAS v2f*)(L + LDS_VEC2))[opr];
        v4u dv[3];
#pragma unroll 1
        for (unsigned sp = 0; sp < SPIN_CAP; ++sp) {
          MEMCLOB();
#pragma unroll
          for (int i = 0; i < 3; ++i) dv[i] = __builtin_amdgcn_raw_buffer_load_b128(rw, WS_DP + r * (D * 8) + opr * 16, i * 8 * D * 8, H4A);
          if (!__any((bad4(dv[0], tag) | bad4(dv[1], tag) | bad4(dv[2], tag)) != 0u)) break;
          if (SLD) __builtin_amdgcn_s_sleep(SLD);
        }
        STAMP(10);
        float z0 = (f32(dv[0].x) + f32(dv[1].x)) + f32(dv[2].x), z1 = (f32(dv[0].z) + f32(dv[1].z)) + f32(dv[2].z);
        z0 += dpp<XOR8>(z0); z1 += dpp<XOR8>(z1);
        z0 = fold16(z0, z0); z1 = fold16(z1, z1); z0 = fold32(z0, z0); z1 = fold32(z1, z1);
        const unsigned o = pack2(x2v.x + rbf(lo16(ma.x) * rbf(z0)), x2v.y + rbf(hi16(ma.x) * rbf(z1)));
        __builtin_amdgcn_raw_buffer_store_b32(o, mkrs(out, D * 2), moff(lane < 8, opr * 4), 0, OAUX);
        if (f == 0 && w == 5) stsc1(tag, rw, moff(lane == 0, WS_EP));
        STAMP(11);
      }
    }
  }
}

extern "C" void kernel_launch(void* const* d_in, const int* in_sizes, int n_in, void* d_out, int out_size, void* d_ws, size_t ws_size,
                              hipStream_t stream) {
  hipLaunchKernelGGL(mega, dim3(256), dim3(512), 0, stream, (const u16*)d_in[0], (const u16*)d_in[1], (const u16*)d_in[2], (const u16*)d_in[3],
                     (const u16*)d_in[4], (const u16*)d_in[6], (const u16*)d_in[7], (const u16*)d_in[8], (const u16*)d_in[9], (const u16*)d_in[10],
                     (u16*)d_out, (unsigned char*)d_ws);
}
```

```cpp
#include <hip/hip_runtime.h>
#include <hip/hip_bf16.h>
#include <cstdint>

typedef unsigned short u16;
typedef unsigned v4u __attribute__((ext_vector_type(4)));
typedef unsigned v2u __attribute__((ext_vector_type(2)));
typedef float v4f __attribute__((ext_vector_type(4)));
typedef float v2f __attribute__((ext_vector_type(2)));
typedef __bf16 bf16x2 __attribute__((ext_vector_type(2)));
typedef short v4s __attribute__((ext_vector_type(4)));
typedef __bf16 bf16x8 __attribute__((ext_vector_type(8)));
typedef __amdgpu_buffer_rsrc_t rsrc_t;
#define LAS __attribute__((address_space(3)))
#define GAS __attribute__((address_space(1)))
#define DEV __device__ __forceinline__

#define PFKB 24
constexpr int CP = 5;
constexpr int SP = 2, SP2 = 5, BW = 3;
constexpr int TAU = 76, TA0 = -30;

constexpr int D = 640, SK = 4096, DH = 192, FF = 1536;
constexpr int AUX_NT = 2, AUX_SC1 = 16;
constexpr int WS_EP = 0, WS_MODG = 4096, WS_QG = 32768, WS_MLG = 65536, WS_OP = 131072, WS_WP = 2 << 20, WP_REP = 40960, WS_SAG = 98304, WS_DP = 786432, WS_BYTES = 3 << 20;
constexpr unsigned SPIN_CAP = 1u << 17;
constexpr float LOG2E = 1.4426950408889634f;
constexpr float QSCALE = 0.07216878364870322f;

constexpr int LDS_RED = 0;
constexpr int LDS_VEC = 6144;
constexpr int LDS_VEC2 = 8704;
constexpr int LDS_HB = 11264;
constexpr int LDS_QS = 12800;
constexpr int LDS_BIG = 16896;
constexpr int LDS_BYTES = 16896 + 81920;

DEV float lo16(unsigned w) { return __uint_as_float(w << 16); }
DEV float hi16(unsigned w) { return __uint_as_float(w & 0xffff0000u); }
DEV float f32(unsigned w) { return __uint_as_float(w); }
DEV unsigned u32(float f) { return __float_as_uint(f); }
DEV float rbf(float f) { return (float)(__bf16)f; }
DEV unsigned pack2(float a, float b) { bf16x2 v = {(__bf16)a, (__bf16)b}; return __builtin_bit_cast(unsigned, v); }
DEV float dot2(unsigned a, unsigned b, float acc) { return __builtin_amdgcn_fdot2_f32_bf16(__builtin_bit_cast(bf16x2, a), __builtin_bit_cast(bf16x2, b), acc, false); }
template <int CTRL> DEV float dpp(float x) { return __builtin_bit_cast(float, __builtin_amdgcn_mov_dpp(__builtin_bit_cast(int, x), CTRL, 0xf, 0xf, true)); }
constexpr int XOR1 = 0xB1, XOR2 = 0x4E, XOR7 = 0x141, XOR8 = 0x128;
DEV float fold32(float a, float b) { auto s = __builtin_amdgcn_permlane32_swap(u32(a), u32(b), false, false); return f32(s[0]) + f32(s[1]); }
DEV float fold16(float a, float b) { auto s = __builtin_amdgcn_permlane16_swap(u32(a), u32(b), false, false); return f32(s[0]) + f32(s[1]); }
DEV float max32(float a) { auto s = __builtin_amdgcn_permlane32_swap(u32(a), u32(a), false, false); return fmaxf(f32(s[0]), f32(s[1])); }
DEV float max16(float a) { auto s = __builtin_amdgcn_permlane16_swap(u32(a), u32(a), false, false); return fmaxf(f32(s[0]), f32(s[1])); }
DEV float wave_sum(float x) { x += dpp<XOR1>(x); x += dpp<XOR2>(x); x += dpp<XOR7>(x); x += dpp<XOR8>(x); x = fold16(x, x); return fold32(x, x); }
DEV rsrc_t mkrs(const void* p, int bytes) {
  const unsigned long long a = (unsigned long long)p;
  const unsigned long long u = ((unsigned long long)(unsigned)__builtin_amdgcn_readfirstlane((int)(a >> 32)) << 32) | (unsigned)__builtin_amdgcn_readfirstlane((int)a);
  return __builtin_amdgcn_make_buffer_rsrc((void*)u, 0, __builtin_amdgcn_readfirstlane(bytes), 0x00020000);
}
DEV v4u ldnt(rsrc_t rs, int voff, int soff) { return __builtin_amdgcn_raw_buffer_load_b128(rs, voff, soff, AUX_NT); }
#define MEMCLOB() asm volatile("" ::: "memory")
DEV v4u ldsc4(rsrc_t rs, int voff, int soff = 0) { return __builtin_amdgcn_raw_buffer_load_b128(rs, voff, soff, AUX_SC1); }
DEV v2u ldsc2(rsrc_t rs, int voff, int soff = 0) { return __builtin_amdgcn_raw_buffer_load_b64(rs, voff, soff, AUX_SC1); }
DEV unsigned ldsc1(rsrc_t rs, int voff) { return __builtin_amdgcn_raw_buffer_load_b32(rs, voff, 0, AUX_SC1); }
DEV void stsc4(v4u v, rsrc_t rs, int voff) { __builtin_amdgcn_raw_buffer_store_b128(v, rs, voff, 0, AUX_SC1); }
DEV void stsc2(v2u v, rsrc_t rs, int voff) { __builtin_amdgcn_raw_buffer_store_b64(v, rs, voff, 0, AUX_SC1); }
DEV void stsc1(unsigned v, rsrc_t rs, int voff) { __builtin_amdgcn_raw_buffer_store_b32(v, rs, voff, 0, AUX_SC1); }
DEV int moff(bool on, int off) { return on ? off : 0x40000000; }
DEV v4u tag4(unsigned tag) { return v4u{tag & 0xffu, (tag >> 8) & 0xffu, (tag + (tag >> 16)) & 0xffu, tag >> 24}; }
DEV unsigned pk24(float f, unsigned t8) { const unsigned u = u32(f); return ((u + 0x7fu + ((u >> 8) & 1u)) & 0xffffff00u) | t8; }
DEV unsigned bad4(v4u v, unsigned tag) { return (v.y ^ tag) | (v.w ^ tag); }
#define PIN24(a) asm volatile("" : "+v"(a[0]), "+v"(a[1]), "+v"(a[2]), "+v"(a[3]), "+v"(a[4]), "+v"(a[5]), "+v"(a[6]), "+v"(a[7]), "+v"(a[8]), "+v"(a[9]), "+v"(a[10]), "+v"(a[11]), \
  "+v"(a[12]), "+v"(a[13]), "+v"(a[14]), "+v"(a[15]), "+v"(a[16]), "+v"(a[17]), "+v"(a[18]), "+v"(a[19]), "+v"(a[20]), "+v"(a[21]), "+v"(a[22]), "+v"(a[23]))
template <int N> DEV void waitvm() { asm volatile("s_waitcnt vmcnt(%0)" ::"i"(N) : "memory"); }
#define SB() __builtin_amdgcn_sched_barrier(0)
DEV void lds_barrier() { asm volatile("s_waitcnt lgkmcnt(0)" ::: "memory"); __builtin_amdgcn_s_barrier(); asm volatile("" ::: "memory"); }
DEV void lds_fence() { asm volatile("s_waitcnt lgkmcnt(0)" ::: "memory"); }

DEV float sum8(float x) { x += dpp<XOR1>(x); x += dpp<XOR2>(x); x += dpp<XOR7>(x); return x; }
DEV v4u trfrag(const LAS unsigned char* p0, const LAS unsigned char* p1) {
  const v2u a = __builtin_bit_cast(v2u, __builtin_amdgcn_ds_read_tr16_b64_v4i16((LAS v4s*)p0)), b = __builtin_bit_cast(v2u, __builtin_amdgcn_ds_read_tr16_b64_v4i16((LAS v4s*)p1));
  v4u o = {a.x, a.y, b.x, b.y};
  asm volatile("" : "+v"(o));
  return o;
}
DEV v4f mfma16(v4u a, v4u b, v4f c) { return __builtin_amdgcn_mfma_f32_16x16x32_bf16(__builtin_bit_cast(bf16x8, a), __builtin_bit_cast(bf16x8, b), c, 0, 0, 0); }
DEV void pairperm(v4u e, v4u o, unsigned (&p)[8]) {
#pragma unroll
  for (int k = 0; k < 4; ++k) { p[2 * k] = __builtin_amdgcn_perm(o[k], e[k], 0x05040100u); p[2 * k + 1] = __builtin_amdgcn_perm(o[k], e[k], 0x07060302u); }
  asm volatile("" : "+v"(p[0]), "+v"(p[1]), "+v"(p[2]), "+v"(p[3]), "+v"(p[4]), "+v"(p[5]), "+v"(p[6]), "+v"(p[7]));
}
DEV void fma8(float (&acc)[24], int base, float p, v4u v) {
#pragma unroll
  for (int k = 0; k < 4; ++k) { acc[base + 2 * k] = fmaf(p, lo16(v[k]), acc[base + 2 * k]); acc[base + 2 * k + 1] = fmaf(p, hi16(v[k]), acc[base + 2 * k + 1]); }
}
DEV void unit_prep(const v4u (&raw)[10], unsigned (&p)[5][8]) {
#pragma unroll
  for (int i = 0; i < 5; ++i) pairperm(raw[2 * i], raw[2 * i + 1], p[i]);
}
DEV void unit_partial(const unsigned (&p)[5][8], LAS unsigned char* L, int vecoff, int redoff, int w, int lane) {
  const LAS unsigned* sc = (const LAS unsigned*)(L + vecoff) + 40 * w + (lane >> 3);
  float a[8];
#pragma unroll
  for (int k = 0; k < 8; ++k) a[k] = 0.f;
#pragma unroll
  for (int i = 0; i < 5; ++i) { const unsigned hv = sc[8 * i];
#pragma unroll
    for (int k = 0; k < 8; ++k) a[k] = dot2(p[i][k], hv, a[k]); }
  float y[4], z[2];
#pragma unroll
  for (int i = 0; i < 4; ++i) y[i] = fold32(a[i], a[i + 4]);
#pragma unroll
  for (int i = 0; i < 2; ++i) z[i] = fold16(y[i], y[i + 2]);
  const float t0 = z[0] + dpp<XOR8>(z[0]), t1 = z[1] + dpp<XOR8>(z[1]);
  ((LAS float*)(L + redoff))[w * 64 + (lane & 7) * 8 + (lane >> 3)] = (lane & 8) ? t1 : t0;
}
DEV v4u ldp(rsrc_t rs, int voff, int soff) { waitvm<CP>(); const v4u v = ldnt(rs, voff, soff); SB(); return v; }
DEV void unit_publish(LAS unsigned char* L, int redoff, int lane, int outoff, unsigned tag, rsrc_t rw) {
  const LAS v2f* red = (const LAS v2f*)(L + redoff) + (lane & 31);
  v2f s = red[0];
#pragma unroll
  for (int i = 1; i < 8; ++i) s += red[i * 32];
  stsc2(v2u{pack2(s.x, s.y), tag}, rw, moff(lane < 32, outoff + lane * 8));
}
template <int LO, int HI, bool PACED> DEV void unit_load(v4u (&raw)[10], const u16* wmod, const u16* wq, bool is_mod, int g, int w, int lane) {
  const int stride = is_mod ? 6 * D * 2 : 32 * DH * 2;
  const rsrc_t rs = mkrs((const unsigned char*)(is_mod ? wmod : wq) + (size_t)80 * w * stride, 80 * stride);
  const int vo = (lane >> 3) * 2 * stride + g * 128 + (lane & 7) * 16;
#pragma unroll
  for (int i = LO; i < HI; ++i) raw[i] = (PACED ? ldp(rs, vo, (16 * (i >> 1) + (i & 1)) * stride) : ldnt(rs, vo, (16 * (i >> 1) + (i & 1)) * stride));
  SB();
}

__global__ void __launch_bounds__(512, 2)
mega(const u16* x, const u16* Kc, const u16* Vc, const u16* wq, const u16* wo,
     const u16* wg, const u16* wu, const u16* wd, const u16* cvec, const u16* wmod,
     u16* out, unsigned char* ws) {
  __shared__ __attribute__((aligned(16))) unsigned char smem[LDS_BYTES];
  LAS unsigned char* L = (LAS unsigned char*)smem;
  const int px = blockIdx.x & 7, pk = blockIdx.x >> 3;
  const int tid = threadIdx.x, lane = tid & 63, w = __builtin_amdgcn_readfirstlane(tid >> 6);
  const int r = lane >> 3, c = lane & 7;
  v4u pf[4];
  if (pk < 8) {
    const rsrc_t rc = mkrs((const void*)(__builtin_amdgcn_s_getpc() & ~127ull), PFKB * 1024);
#pragma unroll
    for (int i = 0; i < 4; ++i) pf[i] = __builtin_amdgcn_raw_buffer_load_b128(rc, tid * 16, i * 8192, 0);
    MEMCLOB();
  }
  const int ph_ = pk >= 20 ? 1 : 0, P = 2 * px + ph_, cg = pk - 8 - 12 * ph_, hq = cg >= 6 ? 1 : 0, s = cg - 6 * hq, h = 2 * P + hq;
  const int rem = s < 2 ? 6 : 5, t0cu = 85 * s + (s < 2 ? s : 2);
  const int nt = w < rem ? 11 : 10, TS = 8 * 3072;
  int nrec = (80 + rem - w) * 3072;
  unsigned kvoff = (unsigned)(h * 512 + t0cu + w) * 3072u;
  asm volatile("" : "+s"(nrec), "+s"(kvoff));
  asm volatile("" ::"s"(x), "s"(Kc), "s"(Vc), "s"(wq), "s"(wo), "s"(wg), "s"(wu), "s"(wd), "s"(cvec), "s"(wmod), "s"(out), "s"(ws));
  const rsrc_t rw = mkrs(ws, WS_BYTES);
  if (__builtin_expect(pk >= 8, 1)) {
    const rsrc_t rk = mkrs((const unsigned char*)Kc + kvoff, nrec), rv = mkrs((const unsigned char*)Vc + kvoff, nrec);
    const rsrc_t rwo = mkrs(wo + (size_t)(384 * P + 48 * w) * D + 64 * cg, cg < 10 ? 48 * D * 2 - 128 * cg : 0);
    const int voff = r * 384 + c * 16, l48 = lane < 48 ? lane : 47;
    v4u kr[11][3], vr[11][3];
    unsigned tag;
    const unsigned tst = (unsigned)__builtin_amdgcn_s_memrealtime() + TA0;
#define PACE(tt, lo) do { waitvm<(lo) + BW>(); if ((int)((unsigned)__builtin_amdgcn_s_memrealtime() - tst - (tt)) < 0) waitvm<lo>(); } while (0)
#define LDV(t) do { PACE(10 * TAU + 2 * TAU / 3 + (t) * TAU, SP); _Pragma("unroll") for (int j = 0; j < 3; ++j) vr[t][j] = ldnt(rv, voff + j * 128, (t) * TS); SB(); } while (0)
#define LDVN(t) do { PACE(10 * TAU + 2 * TAU / 3 + (t) * TAU, SP2); _Pragma("unroll") for (int j = 0; j < 3; ++j) vr[t][j] = ldnt(rv, voff + j * 128, (t) * TS); SB(); } while (0)
#define VPERM(u) do { _Pragma("unroll") for (int j = 0; j < 3; ++j) pairperm(vr[2 * (u)][j], vr[2 * (u) + 1][j], vp[u][j]); SB(); } while (0)
#define PV(u) do { _Pragma("unroll") for (int j = 0; j < 3; ++j) _Pragma("unroll") for (int k = 0; k < 8; ++k) acc[j * 8 + k] = dot2(vp[u][j][k], pp[u], acc[j * 8 + k]); PIN24(acc); SB(); } while (0)
#pragma unroll
    for (int t = 0; t < 11; ++t) {
      if (t >= 2) PACE(t * TAU, SP);
      if (t == 8) tag = ldsc1(rw, WS_EP);
#pragma unroll
      for (int j = 0; j < 3; ++j) { kr[t][j] = ldnt(rk, voff + j * 128, t * TS); SB(); }
    }
    unsigned vp[5][3][8];
    v4u qg;
#pragma unroll
    for (int t = 0; t <= 6; ++t) {
      if (t == 5) { qg = ldsc4(rw, WS_QG + h * 768 + l48 * 16); SB(); }
      LDV(t);
      if (t == 0) { tag += 1u; asm volatile("" : "+v"(tag)); SB(); }
      if (t == 3) VPERM(0);
      if (t == 5) VPERM(1);
    }
    if (__any(bad4(qg, tag) != 0u)) {
#pragma unroll 1
      for (unsigned sp = 0; sp < SPIN_CAP; ++sp) {
        MEMCLOB();
        qg = ldsc4(rw, WS_QG + h * 768 + l48 * 16);
        if (!__any(bad4(qg, tag) != 0u)) break;
        __builtin_amdgcn_s_sleep(2);
      }
    }
    LAS unsigned* qs = (LAS unsigned*)(L + LDS_QS) + w * 96;
    *(LAS v2u*)(qs + 2 * l48) = v2u{qg.x, qg.z};
    lds_fence();
    v4u qp[3];
#pragma unroll
    for (int j = 0; j < 3; ++j) qp[j] = *(const LAS v4u*)(qs + j * 32 + c * 4);
    float sc[12];
#pragma unroll
    for (int t = 0; t < 11; ++t) {
      float d = 0.f;
#pragma unroll
      for (int j = 0; j < 3; ++j)
#pragma unroll
        for (int k = 0; k < 4; ++k) d = dot2(kr[t][j][k], qp[j][k], d);
      d += dpp<XOR1>(d); d += dpp<XOR2>(d); d += dpp<XOR7>(d);
      d = rbf(rbf(d) * QSCALE);
      sc[t] = (t < 10 || nt == 11) ? d : -INFINITY;
      asm volatile("" : "+v"(sc[t]));
      if (t == 4) { SB(); LDVN(7); }
      if (t == 10) { SB(); LDVN(8); }
    }
    float mw = sc[0];
#pragma unroll
    for (int t = 1; t < 11; ++t) mw = fmaxf(mw, sc[t]);
    mw = fmaxf(mw, dpp<XOR8>(mw)); mw = max16(mw); mw = max32(mw);
    float lw = 0.f;
#pragma unroll
    for (int t = 0; t < 11; ++t) { sc[t] = __builtin_amdgcn_exp2f((sc[t] - mw) * LOG2E); lw += sc[t]; }
    sc[11] = 0.f;
    lw += dpp<XOR8>(lw); lw = fold16(lw, lw); lw = fold32(lw, lw);
    unsigned pp[6];
#pragma unroll
    for (int u = 0; u < 6; ++u) pp[u] = pack2(sc[2 * u], sc[2 * u + 1]);
    ((LAS v2f*)(L + LDS_HB))[w] = v2f{mw, lw};
    float acc[24];
#pragma unroll
    for (int i = 0; i < 24; ++i) acc[i] = 0.f;
    v4u wr[6];
#define LDWO() do { _Pragma("unroll") for (int i = 0; i < 6; ++i) wr[i] = ldnt(rwo, r * (2 * D * 2) + c * 16, (16 * (i >> 1) + (i & 1)) * D * 2); SB(); } while (0)
    float fw;
#define MLPUB() do { \
      lds_barrier(); \
      const v2f ml = ((const LAS v2f*)(L + LDS_HB))[c]; \
      float M = ml.x; M = fmaxf(M, dpp<XOR1>(M)); M = fmaxf(M, dpp<XOR2>(M)); M = fmaxf(M, dpp<XOR7>(M)); \
      const float Lc = sum8(ml.y * __builtin_amdgcn_exp2f((ml.x - M) * LOG2E)); \
      fw = __builtin_amdgcn_exp2f((mw - M) * LOG2E); \
      asm volatile("" : "+v"(fw)); \
      if (w == 7) stsc4(v4u{u32(M), tag, u32(Lc), tag}, rw, moff(lane < 8, WS_MLG + (h * 8 + s) * 128 + lane * 16)); \
      SB(); } while (0)
    const int hh = 2 * P + (w >> 2), mloff = moff(lane < 8, WS_MLG + (hh * 8 + (c < 6 ? c : 5)) * 128);
    MLPUB(); LDVN(9); PV(0); PV(1); VPERM(2); PV(2); LDVN(10); VPERM(3); PV(3);
    VPERM(4); PV(4);
    LDWO();
#pragma unroll
    for (int j = 0; j < 3; ++j) fma8(acc, j * 8, lo16(pp[5]), vr[10][j]);
    PIN24(acc); SB();
    {
      float y[12], z[6];
#pragma unroll
      for (int i = 0; i < 12; ++i) y[i] = fold32(acc[i], acc[i + 12]);
#pragma unroll
      for (int i = 0; i < 6; ++i) z[i] = fold16(y[i], y[i + 6]);
      LAS float* red = (LAS float*)(L + LDS_RED) + w * 192 + c * 24 + r * 3;
#pragma unroll
      for (int i = 0; i < 3; ++i) { const float t0 = z[i] + dpp<XOR8>(z[i]), t1 = z[i + 3] + dpp<XOR8>(z[i + 3]); red[i] = ((lane & 8) ? t1 : t0) * fw; }
    }
    v4u ml = ldsc4(rw, mloff);
    lds_barrier();
    if (w < 2) {
      const int dp = 48 * w + l48;
      const LAS v2f* red = (const LAS v2f*)(L + LDS_RED) + ((dp >> 2) & 7) * 12 + (dp >> 5) * 4 + (dp & 3);
      v2f t[8];
#pragma unroll
      for (int i = 0; i < 8; ++i) t[i] = red[i * 96];
      const v2f o = ((t[0] + t[1]) + (t[2] + t[3])) + ((t[4] + t[5]) + (t[6] + t[7]));
      stsc4(v4u{u32(o.x), tag, u32(o.y), tag}, rw, moff(lane < 48, WS_OP + (h * 8 + s) * 2048 + dp * 16));
    }
    SB();
    if (cg < 10) {
      const int hf = lane >> 5, p24 = (lane & 31) < 24 ? (lane & 31) : 23;
      const int opoff = WS_OP + (hh * 8 + 3 * hf) * 2048 + (24 * (w & 3) + p24) * 16;
      v4u op[3];
      unsigned wp[3][8];
      if (__any(lane < 8 && bad4(ml, tag) != 0u)) {
#pragma unroll 1
        for (unsigned sp = 0; sp < SPIN_CAP; ++sp) {
          MEMCLOB();
          ml = ldsc4(rw, mloff);
          if (!__any(lane < 8 && bad4(ml, tag) != 0u)) break;
          __builtin_amdgcn_s_sleep(2);
        }
      }
      float wv;
      {
        float M = f32(ml.x); M = fmaxf(M, dpp<XOR1>(M)); M = fmaxf(M, dpp<XOR2>(M)); M = fmaxf(M, dpp<XOR7>(M));
        const float e = c < 6 ? __builtin_amdgcn_exp2f((f32(ml.x) - M) * LOG2E) : 0.f;
        wv = e / sum8(e * f32(ml.z));
        asm volatile("" : "+v"(wv));
      }
#pragma unroll
      for (int i = 0; i < 3; ++i) op[i] = ldsc4(rw, opoff, i * 2048);
      SB();
      float w3[3];
#pragma unroll
      for (int i = 0; i < 3; ++i) w3[i] = f32((unsigned)__builtin_amdgcn_ds_bpermute((3 * hf + i) * 4, (int)u32(wv)));
#pragma unroll
      for (int i = 0; i < 3; ++i) pairperm(wr[2 * i], wr[2 * i + 1], wp[i]);
      asm volatile("" : "+v"(w3[0]), "+v"(w3[1]), "+v"(w3[2]));
      if (__any((bad4(op[0], tag) | bad4(op[1], tag) | bad4(op[2], tag)) != 0u)) {
#pragma unroll 1
        for (unsigned sp = 0; sp < SPIN_CAP; ++sp) {
          MEMCLOB();
#pragma unroll
          for (int i = 0; i < 3; ++i) op[i] = ldsc4(rw, opoff, i * 2048);
          if (!__any((bad4(op[0], tag) | bad4(op[1], tag) | bad4(op[2], tag)) != 0u)) break;
          __builtin_amdgcn_s_sleep(1);
        }
      }
      unsigned ov[3];
      {
        float o0 = fmaf(w3[2], f32(op[2].x), fmaf(w3[1], f32(op[1].x), w3[0] * f32(op[0].x))), o1 = fmaf(w3[2], f32(op[2].z), fmaf(w3[1], f32(op[1].z), w3[0] * f32(op[0].z)));
        o0 = fold32(o0, o0); o1 = fold32(o1, o1);
        const int opk = (int)pack2(o0, o1);
#pragma unroll
        for (int i = 0; i < 3; ++i) ov[i] = (unsigned)__builtin_amdgcn_ds_bpermute((8 * i + r) * 4, opk);
      }
      float a[8];
#pragma unroll
      for (int k = 0; k < 8; ++k) a[k] = dot2(wp[2][k], ov[2], dot2(wp[1][k], ov[1], dot2(wp[0][k], ov[0], 0.f)));
      float y[4], z[2];
#pragma unroll
      for (int i = 0; i < 4; ++i) y[i] = fold32(a[i], a[i + 4]);
#pragma unroll
      for (int i = 0; i < 2; ++i) { z[i] = fold16(y[i], y[i + 2]); z[i] += dpp<XOR8>(z[i]); }
      ((LAS float*)(L + LDS_VEC))[w * 64 + c * 8 + r] = (lane & 8) ? z[1] : z[0];
      lds_barrier();
      if (w == 2) {
        const LAS v4f* red = (const LAS v4f*)(L + LDS_VEC) + (lane & 15);
        v4f t[8];
#pragma unroll
        for (int i = 0; i < 8; ++i) t[i] = red[i * 16];
        const v4f o = ((t[0] + t[1]) + (t[2] + t[3])) + ((t[4] + t[5]) + (t[6] + t[7]));
        const v4u t4 = tag4(tag), ov4 = {pk24(o.x, t4.x), pk24(o.y, t4.y), pk24(o.z, t4.z), pk24(o.w, t4.w)};
#pragma unroll
        for (int q = 0; q < 2; ++q)
          __builtin_amdgcn_raw_buffer_store_b128(ov4, rw, WS_WP + (lane >> 4) * WP_REP + (P * D + 64 * cg + 4 * (lane & 15)) * 4, q * 4 * WP_REP, AUX_SC1);
      }
    }
  } else {
    const int b = blockIdx.x, xg = px, v = pk;
    const int t320 = tid < 320 ? tid : 319;
    unsigned tag, cpair;
#define LDC() do { MEMCLOB(); cpair = ((const GAS unsigned*)cvec)[t320]; tag = ldsc1(rw, WS_EP); SB(); } while (0)
    unsigned xpair, xall[5];
    float ln0, ln1;
    v4u u0[10], u1[10], sg[4];
    unsigned p0[5][8], p1[5][8];
    const int rp = lane & 31, hf = lane >> 5;
#define FASTG(p) ((p) + ((p) + 4) / 7)
#define LDX() do { xpair = ((const GAS unsigned*)x)[t320]; _Pragma("unroll") for (int i = 0; i < 5; ++i) xall[i] = ((const GAS unsigned*)x)[lane + 64 * i]; SB(); } while (0)
#define SILU() do { asm volatile("" ::"v"(pf[0]), "v"(pf[1]), "v"(pf[2]), "v"(pf[3])); tag += 1u; const float c0 = lo16(cpair), c1 = hi16(cpair); \
      ((LAS unsigned*)(L + LDS_VEC))[t320] = pack2(c0 / (1.f + expf(-c0)), c1 / (1.f + expf(-c1))); SB(); } while (0)
#define LNX() do { float sm = 0.f; \
      _Pragma("unroll") for (int i = 0; i < 5; ++i) sm += lo16(xall[i]) + hi16(xall[i]); \
      const float mean = wave_sum(sm) * (1.f / D); float sq = 0.f; \
      _Pragma("unroll") for (int i = 0; i < 5; ++i) { const float a = lo16(xall[i]) - mean, bb = hi16(xall[i]) - mean; sq += a * a + bb * bb; } \
      const float var = wave_sum(sq) * (1.f / D); \
      const float mb = rbf(mean), sd = rbf(sqrtf(rbf(rbf(var) + 1e-5f))); \
      ln0 = rbf(rbf(lo16(xpair) - mb) / sd); ln1 = rbf(rbf(hi16(xpair) - mb) / sd); \
      asm volatile("" : "+v"(ln0), "+v"(ln1)); SB(); } while (0)
#define LDSAG() do { _Pragma("unroll") for (int i = 0; i < 4; ++i) sg[i] = ldsc4(rw, moff(tid < 320, WS_SAG + tid * 16), (i & 1) * 10240 + (i >> 1) * 5120); } while (0)
#define MAKEH() do { if (w < 5) { \
      if (__any((bad4(sg[0], tag) | bad4(sg[1], tag) | bad4(sg[2], tag) | bad4(sg[3], tag)) != 0u)) { \
        _Pragma("unroll 1") for (unsigned sp = 0; sp < SPIN_CAP; ++sp) { \
          MEMCLOB(); LDSAG(); \
          if (!__any((bad4(sg[0], tag) | bad4(sg[1], tag) | bad4(sg[2], tag) | bad4(sg[3], tag)) != 0u)) break; \
          __builtin_amdgcn_s_sleep(1); } } \
      const float sa0 = rbf(f32(sg[0].x) + f32(sg[1].x)), sa1 = rbf(f32(sg[0].z) + f32(sg[1].z)); \
      const float ca0 = rbf(f32(sg[2].x) + f32(sg[3].x)), ca1 = rbf(f32(sg[2].z) + f32(sg[3].z)); \
      ((LAS unsigned*)(L + LDS_VEC2))[tid] = pack2(rbf(ln0 * rbf(1.f + ca0)) + sa0, rbf(ln1 * rbf(1.f + ca1)) + sa1); } \
      lds_barrier(); } while (0)
    if (b < 40) {
      const int wi = 5 * xg + v, ga_ = wi < 20 ? wi : wi - 20, ha_ = wi < 20 ? 0 : 1, uu = b - 8;
      const bool two = v == 0 || (uu >= 12 && uu < 18);
      const int g0 = v == 0 ? FASTG(20 + xg) : (uu < 12 ? 8 * uu + 3 : FASTG(uu - 12));
      const int g1 = v == 0 ? FASTG(28 + xg) : FASTG(24 + uu);
      v4u ua[6];
      LDC();
      LDX();
      {
        const rsrc_t ra = mkrs(wmod + (size_t)(320 * ha_ + 40 * w) * (6 * D), 40 * 6 * D * 2);
#pragma unroll
        for (int i = 0; i < 6; ++i) ua[i] = ldnt(ra, 2 * r * (6 * D * 2) + ga_ * 128 + c * 16, (16 * (i >> 1) + (i & 1)) * (6 * D * 2));
        SB();
      }
      SILU();
      lds_barrier();
      unsigned hv[3];
      {
        const LAS unsigned* sc = (const LAS unsigned*)(L + LDS_VEC) + 160 * ha_ + 20 * w + r;
        hv[0] = sc[0]; hv[1] = sc[8]; hv[2] = sc[16]; hv[2] = r < 4 ? hv[2] : 0u;
      }
      unit_load<0, 5, true>(u0, wmod, wq, false, g0, w, lane);
      {
        unsigned pa[3][8];
#pragma unroll
        for (int i = 0; i < 3; ++i) pairperm(ua[2 * i], ua[2 * i + 1], pa[i]);
        float a[8];
#pragma unroll
        for (int k = 0; k < 8; ++k) a[k] = dot2(pa[2][k], hv[2], dot2(pa[1][k], hv[1], dot2(pa[0][k], hv[0], 0.f)));
        float y[4], z[2];
#pragma unroll
        for (int i = 0; i < 4; ++i) y[i] = fold32(a[i], a[i + 4]);
#pragma unroll
        for (int i = 0; i < 2; ++i) z[i] = fold16(y[i], y[i + 2]);
        const float t0 = z[0] + dpp<XOR8>(z[0]), t1 = z[1] + dpp<XOR8>(z[1]);
        ((LAS float*)(L + LDS_RED))[w * 64 + c * 8 + r] = (lane & 8) ? t1 : t0;
        lds_barrier();
        if (w == 0) {
          const LAS v2f* red = (const LAS v2f*)(L + LDS_RED) + (lane & 31);
          v2f sm = red[0];
#pragma unroll
          for (int i = 1; i < 8; ++i) sm += red[i * 32];
          stsc4(v4u{u32(sm.x), tag, u32(sm.y), tag}, rw, moff(lane < 32, WS_SAG + ha_ * 10240 + ga_ * 512 + lane * 16));
        }
      }
      SB();
      unit_load<5, 10, true>(u0, wmod, wq, false, g0, w, lane);
      LNX();
      if (two) unit_load<0, 10, true>(u1, wmod, wq, false, g1, w, lane);
      waitvm<2>();
      LDSAG();
      SB();
      unit_prep(u0, p0);
      if (two) unit_prep(u1, p1);
      SB();
      MAKEH();
      unit_partial(p0, L, LDS_VEC2, LDS_RED + 2048, w, lane);
      if (two) unit_partial(p1, L, LDS_VEC2, LDS_RED + 4096, w, lane);
      lds_barrier();
      if (w == 0 || (w == 1 && two)) unit_publish(L, LDS_RED + 2048 + w * 2048, lane, WS_QG + (w ? g1 : g0) * 256, tag, rw);
      SB();
      unit_load<0, 10, true>(u0, wmod, wq, true, 20 + b, w, lane);
      unit_prep(u0, p0);
      unit_partial(p0, L, LDS_VEC, LDS_RED, w, lane);
      lds_barrier();
      if (w == 0) unit_publish(L, LDS_RED, lane, WS_MODG + (20 + b) * 256, tag, rw);
    } else {
      const int f = 3 * px + pk - 5, fi = f - ((f + 4) >> 3);
      const int g0 = FASTG(42 + fi), g1 = FASTG(63 + fi);
      LDC();
      LDX();
      if ((f & 7) != 3) {
        unit_load<0, 6, false>(u0, wmod, wq, false, g0, w, lane);
        asm volatile("" : "+v"(tag), "+v"(xpair) : "v"(pf[0]), "v"(pf[1]), "v"(pf[2]), "v"(pf[3]), "v"(cpair));
        tag += 1u;
        unit_load<6, 10, true>(u0, wmod, wq, false, g0, w, lane);
        LNX();
        unit_load<0, 8, true>(u1, wmod, wq, false, g1, w, lane);
        unit_prep(u0, p0);
        unit_load<8, 10, true>(u1, wmod, wq, false, g1, w, lane);
        waitvm<2>();
        LDSAG();
        SB();
        unit_prep(u1, p1);
        SB();
        MAKEH();
        unit_partial(p0, L, LDS_VEC2, LDS_RED + 2048, w, lane);
        unit_partial(p1, L, LDS_VEC2, LDS_RED + 4096, w, lane);
        lds_barrier();
        if (w < 2) unit_publish(L, LDS_RED + 2048 + w * 2048, lane, WS_QG + (w ? g1 : g0) * 256, tag, rw);
        SB();
      } else {
        asm volatile("; no front work" : "+v"(tag), "+v"(xpair) : "v"(pf[0]), "v"(pf[1]), "v"(pf[2]), "v"(pf[3]), "v"(cpair));
        tag += 1u;
      }
      SB();
      const rsrc_t rd = mkrs(wd + (size_t)(f * 64 + 8 * w) * D, 8 * D * 2);
      const rsrc_t rg = mkrs(wg + f * 64, D * FF * 2 - f * 128), ru = mkrs(wu + f * 64, D * FF * 2 - f * 128);
      const int gvo = (40 * w + r) * (FF * 2) + c * 16;
      const int g4 = lane >> 4, q4 = (lane >> 2) & 3, p4 = lane & 3;
      v4u gu[20], dw[5][2];
#define LDD(nn, lo, hi) do { _Pragma("unroll") for (int i = lo; i < hi; ++i) { waitvm<CP>(); nn[i] = ldnt(rd, lane * 16, i * 1024); SB(); } } while (0)
#define LDGU(nn, p, lo, hi) do { _Pragma("unroll") for (int i = lo; i < hi; ++i) { waitvm<CP>(); nn[i] = ldnt(i < 5 ? rg : ru, gvo, (320 * (p) + 8 * (i % 5)) * FF * 2); SB(); } } while (0)
#define GUROUND(p, nn) do { \
        _Pragma("unroll") for (int i = 0; i < 5; ++i) { \
          const int row = 40 * w + 8 * i + r, a = row * 128 + ((c ^ ((row >> 1) & 7)) << 4); \
          *(LAS v4u*)(L + LDS_BIG + a) = nn[i]; *(LAS v4u*)(L + LDS_BIG + 40960 + a) = nn[5 + i]; } \
        lds_barrier(); \
        { const int row0 = 8 * g4 + q4, row1 = row0 + 4; \
          const LAS unsigned char* q0 = L + LDS_BIG + (p4 >> 1) * 40960 + row0 * 128 + ((w ^ ((row0 >> 1) & 7)) << 4) + (p4 & 1) * 8; \
          const LAS unsigned char* q1 = L + LDS_BIG + (p4 >> 1) * 40960 + row1 * 128 + ((w ^ ((row1 >> 1) & 7)) << 4) + (p4 & 1) * 8; \
          _Pragma("unroll") for (int ks = 0; ks < 10; ++ks) gu[(p) * 10 + ks] = trfrag(q0 + ks * 4096, q1 + ks * 4096); } \
        SB(); } while (0)
      LDGU(u0, 0, 0, 10);
      LDGU(u1, 1, 0, 6);
      GUROUND(0, u0);
      LDGU(u1, 1, 6, 10);
      LDD(u0, 0, 6);
      lds_barrier();
      GUROUND(1, u1);
      LDD(u0, 6, 10);
      lds_barrier();
#pragma unroll
      for (int i = 0; i < 10; ++i) *(LAS v4u*)(L + LDS_BIG + (10 * w + i) * 1024 + lane * 16) = u0[i];
      lds_barrier();
      {
        const LAS unsigned char* q0 = L + LDS_BIG + (8 * g4 + q4) * (D * 2) + w * 160 + p4 * 8;
#pragma unroll
        for (int T = 0; T < 5; ++T)
#pragma unroll
          for (int ks = 0; ks < 2; ++ks) dw[T][ks] = trfrag(q0 + ks * 32 * D * 2 + T * 32, q0 + (ks * 32 + 4) * D * 2 + T * 32);
        SB();
      }
      const bool red_on = (w == 5 || w == 6) && f < 20;
      const int opr = 16 * f + 8 * (w - 5) + c;
      int dpo0 = WS_DP + (f * D + 80 * w + lane) * 8, dpo1 = moff(lane < 16, dpo0 + 512), dpr = WS_DP + r * (D * 8) + opr * 16;
      asm volatile("" : "+v"(dpo0), "+v"(dpo1), "+v"(dpr));
      LAS v2f* stat = (LAS v2f*)(L + LDS_QS + 1024);
      float x20 = 0.f, x21 = 0.f;
      v2u ma, mb2, mc;
      const int pi = (tid & ~63) + 2 * rp + hf;
      xpair = (unsigned)__builtin_amdgcn_ds_bpermute((2 * rp + hf) * 4, (int)xpair);
      if (w >= 5) {
        stat[w] = v2f{0.f, 0.f};
        if (red_on) {
#pragma unroll 1
          for (unsigned sp = 0; sp < SPIN_CAP; ++sp) {
            MEMCLOB();
            ma = ldsc2(rw, WS_MODG + (1600 + opr) * 8);
            if (!__any(ma.y != tag)) break;
            __builtin_amdgcn_s_sleep(4);
          }
        }
      } else {
        v4u pv[8];
        unsigned miss = 0x1ffu;
        const int wpoff = WS_WP + px * WP_REP + (hf * D + 128 * w + 4 * rp) * 4;
        const v4u t4 = tag4(tag);
#pragma unroll 1
        for (unsigned sp = 0; sp < SPIN_CAP && miss; ++sp) {
          MEMCLOB();
#pragma unroll
          for (int g = 0; g < 8; ++g) if (__builtin_expect(miss & (1u << g), 0)) pv[g] = ldsc4(rw, wpoff, (2 * g) * D * 4);
          if (__builtin_expect(miss & 256u, 0)) { ma = ldsc2(rw, WS_MODG + (640 + pi) * 8); mb2 = ldsc2(rw, WS_MODG + (960 + pi) * 8); mc = ldsc2(rw, WS_MODG + (1280 + pi) * 8); }
#pragma unroll
          for (int g = 0; g < 8; ++g) if (__builtin_expect(miss & (1u << g), 0)) {
            const v4u e = pv[g] ^ t4;
            if (!__any(((e.x | e.y | e.z | e.w) & 0xffu) != 0u)) {
              miss &= ~(1u << g);
              pv[g].x = u32(fold32(f32(e.x), f32(e.z))); pv[g].y = u32(fold32(f32(e.y), f32(e.w)));
            }
          }
          if (__builtin_expect(miss & 256u, 0)) {
            if (!__any(((ma.y ^ tag) | (mb2.y ^ tag) | (mc.y ^ tag)) != 0u)) { miss &= 255u; mc.x = pack2(1.f + lo16(mc.x), 1.f + hi16(mc.x)); }
          }
        }
        const v2f at = {((f32(pv[0].x) + f32(pv[1].x)) + (f32(pv[2].x) + f32(pv[3].x))) + ((f32(pv[4].x) + f32(pv[5].x)) + (f32(pv[6].x) + f32(pv[7].x))),
                        ((f32(pv[0].y) + f32(pv[1].y)) + (f32(pv[2].y) + f32(pv[3].y))) + ((f32(pv[4].y) + f32(pv[5].y)) + (f32(pv[6].y) + f32(pv[7].y)))};
        x20 = rbf(lo16(xpair) + rbf(lo16(ma.x) * rbf(at.x))); x21 = rbf(hi16(xpair) + rbf(hi16(ma.x) * rbf(at.y)));
        *(LAS v2f*)(L + LDS_VEC2 + pi * 8) = v2f{x20, x21};
        const float piv = __builtin_bit_cast(float, __builtin_amdgcn_readfirstlane(__builtin_bit_cast(int, x20)));
        const float d0 = x20 - piv, d1 = x21 - piv;
        const float s1 = wave_sum(d0 + d1), s2 = wave_sum(d0 * d0 + d1 * d1);
        stat[w] = v2f{piv + s1 * (1.f / 128), s2 - s1 * s1 * (1.f / 128)};
      }
      lds_barrier();
      if (w < 5) {
        const v2f st = stat[c];
        const float mean = sum8(st.x) * 0.2f, dm = st.x - mean;
        const float m2 = sum8(c < 5 ? fmaf(128.f * dm, dm, st.y) : 0.f);
        const float mb = rbf(mean), sd = rbf(__builtin_amdgcn_sqrtf(rbf(rbf(fmaxf(m2, 0.f) * (1.f / D)) + 1e-5f)));
        const float rs = __builtin_amdgcn_rcpf(sd);
        const float l0 = rbf(rbf(x20 - mb) * rs), l1 = rbf(rbf(x21 - mb) * rs);
        ((LAS unsigned*)(L + LDS_HB))[pi] = pack2(rbf(l0 * lo16(mc.x)) + lo16(mb2.x), rbf(l1 * hi16(mc.x)) + hi16(mb2.x));
      }
      lds_barrier();
      {
        const LAS v4u* hb = (const LAS v4u*)(L + LDS_HB) + g4;
        v4u ha[20];
#pragma unroll
        for (int ks = 0; ks < 20; ++ks) ha[ks] = hb[4 * ks];
        SB();
        v4f acc = {0.f, 0.f, 0.f, 0.f};
#pragma unroll
        for (int ks = 0; ks < 20; ++ks) acc = mfma16(ha[ks], gu[ks], acc);
        const float vv = rbf(acc.x);
        ((LAS __bf16*)(L + LDS_QS))[8 * w + c] = (__bf16)(vv * dpp<XOR8>(vv));
      }
      lds_barrier();
      {
        const LAS v4u* hp2 = (const LAS v4u*)(L + LDS_QS) + g4;
        const v4u h0 = hp2[0], h1 = hp2[4];
        v4f acc[5];
#pragma unroll
        for (int T = 0; T < 5; ++T) acc[T] = mfma16(h0, dw[T][0], v4f{0.f, 0.f, 0.f, 0.f});
#pragma unroll
        for (int T = 0; T < 5; ++T) acc[T] = mfma16(h1, dw[T][1], acc[T]);
        const float z = (lane & 32) ? ((lane & 16) ? acc[3].x : acc[2].x) : ((lane & 16) ? acc[1].x : acc[0].x);
        stsc2(v2u{u32(z), tag}, rw, dpo0);
        stsc2(v2u{u32(acc[4].x), tag}, rw, dpo1);
      }
      if (red_on) {
        const v2f x2v = ((const LAS v2f*)(L + LDS_VEC2))[opr];
        v4u dv[3];
#pragma unroll 1
        for (unsigned sp = 0; sp < SPIN_CAP; ++sp) {
          MEMCLOB();
#pragma unroll
          for (int i = 0; i < 3; ++i) dv[i] = __builtin_amdgcn_raw_buffer_load_b128(rw, dpr, i * 8 * D * 8, AUX_SC1);
          if (!__any((bad4(dv[0], tag) | bad4(dv[1], tag) | bad4(dv[2], tag)) != 0u)) break;
        }
        float z0 = (f32(dv[0].x) + f32(dv[1].x)) + f32(dv[2].x), z1 = (f32(dv[0].z) + f32(dv[1].z)) + f32(dv[2].z);
        z0 += dpp<XOR8>(z0); z1 += dpp<XOR8>(z1);
        z0 = fold16(z0, z0); z1 = fold16(z1, z1); z0 = fold32(z0, z0); z1 = fold32(z1, z1);
        const unsigned o = pack2(x2v.x + rbf(lo16(ma.x) * rbf(z0)), x2v.y + rbf(hi16(ma.x) * rbf(z1)));
        __builtin_amdgcn_raw_buffer_store_b32(o, mkrs(out, D * 2), moff(lane < 8, opr * 4), 0, 0);
        if (f == 0 && w == 5) stsc1(tag, rw, moff(lane == 0, WS_EP));
      }
    }
  }
}

extern "C" void kernel_launch(void* const* d_in, const int* in_sizes, int n_in, void* d_out, int out_size, void* d_ws, size_t ws_size,
                              hipStream_t stream) {
  hipLaunchKernelGGL(mega, dim3(256), dim3(512), 0, stream, (const u16*)d_in[0], (const u16*)d_in[1], (const u16*)d_in[2], (const u16*)d_in[3],
                     (const u16*)d_in[4], (const u16*)d_in[6], (const u16*)d_in[7], (const u16*)d_in[8], (const u16*)d_in[9], (const u16*)d_in[10],
                     (u16*)d_out, (unsigned char*)d_ws);
}
```

```cpp
#include <hip/hip_runtime.h>
#include <hip/hip_bf16.h>
#include <cstdint>

typedef unsigned short u16;
typedef unsigned v4u __attribute__((ext_vector_type(4)));
typedef unsigned v2u __attribute__((ext_vector_type(2)));
typedef float v4f __attribute__((ext_vector_type(4)));
typedef float v2f __attribute__((ext_vector_type(2)));
typedef __bf16 bf16x2 __attribute__((ext_vector_type(2)));
typedef short v4s __attribute__((ext_vector_type(4)));
typedef __bf16 bf16x8 __attribute__((ext_vector_type(8)));
typedef __amdgpu_buffer_rsrc_t rsrc_t;
#define LAS __attribute__((address_space(3)))
#define GAS __attribute__((address_space(1)))
#define DEV __device__ __forceinline__

#define PFKB 24
constexpr int CP = 5;
constexpr int SP = 2, SP2 = 5, BW = 3;
constexpr int TAU = 76, TA0 = -30;

constexpr int D = 640, SK = 4096, DH = 192, FF = 1536;
constexpr int AUX_NT = 2, AUX_SC1 = 16;
constexpr int WS_EP = 0, WS_MODG = 4096, WS_QG = 32768, WS_MLG = 65536, WS_OP = 131072, WS_WP = 2 << 20, WP_REP = 40960, WS_SAG = 98304, WS_DP = 786432, WS_BYTES = 2 << 20;
constexpr unsigned SPIN_CAP = 1u << 17;
constexpr float LOG2E = 1.4426950408889634f;
constexpr float QSCALE = 0.07216878364870322f;

constexpr int LDS_RED = 0;
constexpr int LDS_VEC = 6144;
constexpr int LDS_VEC2 = 8704;
constexpr int LDS_HB = 11264;
constexpr int LDS_QS = 12800;
constexpr int LDS_BIG = 16896;
constexpr int LDS_BYTES = 16896 + 81920;

DEV float lo16(unsigned w) { return __uint_as_float(w << 16); }
DEV float hi16(unsigned w) { return __uint_as_float(w & 0xffff0000u); }
DEV float f32(unsigned w) { return __uint_as_float(w); }
DEV unsigned u32(float f) { return __float_as_uint(f); }
DEV float rbf(float f) { return (float)(__bf16)f; }
DEV unsigned pack2(float a, float b) { bf16x2 v = {(__bf16)a, (__bf16)b}; return __builtin_bit_cast(unsigned, v); }
DEV float dot2(unsigned a, unsigned b, float acc) { return __builtin_amdgcn_fdot2_f32_bf16(__builtin_bit_cast(bf16x2, a), __builtin_bit_cast(bf16x2, b), acc, false); }
template <int CTRL> DEV float dpp(float x) { return __builtin_bit_cast(float, __builtin_amdgcn_mov_dpp(__builtin_bit_cast(int, x), CTRL, 0xf, 0xf, true)); }
constexpr int XOR1 = 0xB1, XOR2 = 0x4E, XOR7 = 0x141, XOR8 = 0x128;
DEV float fold32(float a, float b) { auto s = __builtin_amdgcn_permlane32_swap(u32(a), u32(b), false, false); return f32(s[0]) + f32(s[1]); }
DEV float fold16(float a, float b) { auto s = __builtin_amdgcn_permlane16_swap(u32(a), u32(b), false, false); return f32(s[0]) + f32(s[1]); }
DEV float max32(float a) { auto s = __builtin_amdgcn_permlane32_swap(u32(a), u32(a), false, false); return fmaxf(f32(s[0]), f32(s[1])); }
DEV float max16(float a) { auto s = __builtin_amdgcn_permlane16_swap(u32(a), u32(a), false, false); return fmaxf(f32(s[0]), f32(s[1])); }
DEV float wave_sum(float x) { x += dpp<XOR1>(x); x += dpp<XOR2>(x); x += dpp<XOR7>(x); x += dpp<XOR8>(x); x = fold16(x, x); return fold32(x, x); }
DEV rsrc_t mkrs(const void* p, int bytes) {
  const unsigned long long a = (unsigned long long)p;
  const unsigned long long u = ((unsigned long long)(unsigned)__builtin_amdgcn_readfirstlane((int)(a >> 32)) << 32) | (unsigned)__builtin_amdgcn_readfirstlane((int)a);
  return __builtin_amdgcn_make_buffer_rsrc((void*)u, 0, __builtin_amdgcn_readfirstlane(bytes), 0x00020000);
}
DEV v4u ldnt(rsrc_t rs, int voff, int soff) { return __builtin_amdgcn_raw_buffer_load_b128(rs, voff, soff, AUX_NT); }
#define MEMCLOB() asm volatile("" ::: "memory")
DEV v4u ldsc4(rsrc_t rs, int voff, int soff = 0) { return __builtin_amdgcn_raw_buffer_load_b128(rs, voff, soff, AUX_SC1); }
DEV v2u ldsc2(rsrc_t rs, int voff, int soff = 0) { return __builtin_amdgcn_raw_buffer_load_b64(rs, voff, soff, AUX_SC1); }
DEV unsigned ldsc1(rsrc_t rs, int voff) { return __builtin_amdgcn_raw_buffer_load_b32(rs, voff, 0, AUX_SC1); }
DEV void stsc4(v4u v, rsrc_t rs, int voff) { __builtin_amdgcn_raw_buffer_store_b128(v, rs, voff, 0, AUX_SC1); }
DEV void stsc2(v2u v, rsrc_t rs, int voff) { __builtin_amdgcn_raw_buffer_store_b64(v, rs, voff, 0, AUX_SC1); }
DEV void stsc1(unsigned v, rsrc_t rs, int voff) { __builtin_amdgcn_raw_buffer_store_b32(v, rs, voff, 0, AUX_SC1); }
DEV int moff(bool on, int off) { return on ? off : 0x40000000; }
DEV v4u tag4(unsigned tag) { return v4u{tag & 0xffu, (tag >> 8) & 0xffu, (tag + (tag >> 16)) & 0xffu, tag >> 24}; }
DEV unsigned pk24(float f, unsigned t8) { const unsigned u = u32(f); return ((u + 0x7fu + ((u >> 8) & 1u)) & 0xffffff00u) | t8; }
DEV unsigned bad4(v4u v, unsigned tag) { return (v.y ^ tag) | (v.w ^ tag); }
#define PIN24(a) asm volatile("" : "+v"(a[0]), "+v"(a[1]), "+v"(a[2]), "+v"(a[3]), "+v"(a[4]), "+v"(a[5]), "+v"(a[6]), "+v"(a[7]), "+v"(a[8]), "+v"(a[9]), "+v"(a[10]), "+v"(a[11]), \
  "+v"(a[12]), "+v"(a[13]), "+v"(a[14]), "+v"(a[15]), "+v"(a[16]), "+v"(a[17]), "+v"(a[18]), "+v"(a[19]), "+v"(a[20]), "+v"(a[21]), "+v"(a[22]), "+v"(a[23]))
template <int N> DEV void waitvm() { asm volatile("s_waitcnt vmcnt(%0)" ::"i"(N) : "memory"); }
#define SB() __builtin_amdgcn_sched_barrier(0)
DEV void lds_barrier() { asm volatile("s_waitcnt lgkmcnt(0)" ::: "memory"); __builtin_amdgcn_s_barrier(); asm volatile("" ::: "memory"); }
DEV void lds_fence() { asm volatile("s_waitcnt lgkmcnt(0)" ::: "memory"); }

DEV float sum8(float x) { x += dpp<XOR1>(x); x += dpp<XOR2>(x); x += dpp<XOR7>(x); return x; }
DEV v4u trfrag(const LAS unsigned char* p0, const LAS unsigned char* p1) {
  const v2u a = __builtin_bit_cast(v2u, __builtin_amdgcn_ds_read_tr16_b64_v4i16((LAS v4s*)p0)), b = __builtin_bit_cast(v2u, __builtin_amdgcn_ds_read_tr16_b64_v4i16((LAS v4s*)p1));
  v4u o = {a.x, a.y, b.x, b.y};
  asm volatile("" : "+v"(o));
  return o;
}
DEV v4f mfma16(v4u a, v4u b, v4f c) { return __builtin_amdgcn_mfma_f32_16x16x32_bf16(__builtin_bit_cast(bf16x8, a), __builtin_bit_cast(bf16x8, b), c, 0, 0, 0); }
DEV void pairperm(v4u e, v4u o, unsigned (&p)[8]) {
#pragma unroll
  for (int k = 0; k < 4; ++k) { p[2 * k] = __builtin_amdgcn_perm(o[k], e[k], 0x05040100u); p[2 * k + 1] = __builtin_amdgcn_perm(o[k], e[k], 0x07060302u); }
  asm volatile("" : "+v"(p[0]), "+v"(p[1]), "+v"(p[2]), "+v"(p[3]), "+v"(p[4]), "+v"(p[5]), "+v"(p[6]), "+v"(p[7]));
}
DEV void fma8(float (&acc)[24], int base, float p, v4u v) {
#pragma unroll
  for (int k = 0; k < 4; ++k) { acc[base + 2 * k] = fmaf(p, lo16(v[k]), acc[base + 2 * k]); acc[base + 2 * k + 1] = fmaf(p, hi16(v[k]), acc[base + 2 * k + 1]); }
}
DEV void unit_prep(const v4u (&raw)[10], unsigned (&p)[5][8]) {
#pragma unroll
  for (int i = 0; i < 5; ++i) pairperm(raw[2 * i], raw[2 * i + 1], p[i]);
}
DEV void unit_partial(const unsigned (&p)[5][8], LAS unsigned char* L, int vecoff, int redoff, int w, int lane) {
  const LAS unsigned* sc = (const LAS unsigned*)(L + vecoff) + 40 * w + (lane >> 3);
  float a[8];
#pragma unroll
  for (int k = 0; k < 8; ++k) a[k] = 0.f;
#pragma unroll
  for (int i = 0; i < 5; ++i) { const unsigned hv = sc[8 * i];
#pragma unroll
    for (int k = 0; k < 8; ++k) a[k] = dot2(p[i][k], hv, a[k]); }
  float y[4], z[2];
#pragma unroll
  for (int i = 0; i < 4; ++i) y[i] = fold32(a[i], a[i + 4]);
#pragma unroll
  for (int i = 0; i < 2; ++i) z[i] = fold16(y[i], y[i + 2]);
  const float t0 = z[0] + dpp<XOR8>(z[0]), t1 = z[1] + dpp<XOR8>(z[1]);
  ((LAS float*)(L + redoff))[w * 64 + (lane & 7) * 8 + (lane >> 3)] = (lane & 8) ? t1 : t0;
}
DEV v4u ldp(rsrc_t rs, int voff, int soff) { waitvm<CP>(); const v4u v = ldnt(rs, voff, soff); SB(); return v; }
DEV void unit_publish(LAS unsigned char* L, int redoff, int lane, int outoff, unsigned tag, rsrc_t rw) {
  const LAS v2f* red = (const LAS v2f*)(L + redoff) + (lane & 31);
  v2f s = red[0];
#pragma unroll
  for (int i = 1; i < 8; ++i) s += red[i * 32];
  stsc2(v2u{pack2(s.x, s.y), tag}, rw, moff(lane < 32, outoff + lane * 8));
}
template <int LO, int HI, bool PACED> DEV void unit_load(v4u (&raw)[10], const u16* wmod, const u16* wq, bool is_mod, int g, int w, int lane) {
  const int stride = is_mod ? 6 * D * 2 : 32 * DH * 2;
  const rsrc_t rs = mkrs((const unsigned char*)(is_mod ? wmod : wq) + (size_t)80 * w * stride, 80 * stride);
  const int vo = (lane >> 3) * 2 * stride + g * 128 + (lane & 7) * 16;
#pragma unroll
  for (int i = LO; i < HI; ++i) raw[i] = (PACED ? ldp(rs, vo, (16 * (i >> 1) + (i & 1)) * stride) : ldnt(rs, vo, (16 * (i >> 1) + (i & 1)) * stride));
  SB();
}

__global__ void __launch_bounds__(512, 2)
mega(const u16* x, const u16* Kc, const u16* Vc, const u16* wq, const u16* wo,
     const u16* wg, const u16* wu, const u16* wd, const u16* cvec, const u16* wmod,
     u16* out, unsigned char* ws) {
  __shared__ __attribute__((aligned(16))) unsigned char smem[LDS_BYTES];
  LAS unsigned char* L = (LAS unsigned char*)smem;
  const int px = blockIdx.x & 7, pk = blockIdx.x >> 3;
  const int tid = threadIdx.x, lane = tid & 63, w = __builtin_amdgcn_readfirstlane(tid >> 6);
  const int r = lane >> 3, c = lane & 7;
  v4u pf[4];
  if (pk < 8) {
    const rsrc_t rc = mkrs((const void*)(__builtin_amdgcn_s_getpc() & ~127ull), PFKB * 1024);
#pragma unroll
    for (int i = 0; i < 4; ++i) pf[i] = __builtin_amdgcn_raw_buffer_load_b128(rc, tid * 16, i * 8192, 0);
    MEMCLOB();
  }
  const int ph_ = pk >= 20 ? 1 : 0, P = 2 * px + ph_, cg = pk - 8 - 12 * ph_, hq = cg >= 6 ? 1 : 0, s = cg - 6 * hq, h = 2 * P + hq;
  const int rem = s < 2 ? 6 : 5, t0cu = 85 * s + (s < 2 ? s : 2);
  const int nt = w < rem ? 11 : 10, TS = 8 * 3072;
  int nrec = (80 + rem - w) * 3072;
  unsigned kvoff = (unsigned)(h * 512 + t0cu + w) * 3072u;
  asm volatile("" : "+s"(nrec), "+s"(kvoff));
  asm volatile("" ::"s"(x), "s"(Kc), "s"(Vc), "s"(wq), "s"(wo), "s"(wg), "s"(wu), "s"(wd), "s"(cvec), "s"(wmod), "s"(out), "s"(ws));
  const rsrc_t rw = mkrs(ws, WS_BYTES);
  if (__builtin_expect(pk >= 8, 1)) {
    const rsrc_t rk = mkrs((const unsigned char*)Kc + kvoff, nrec), rv = mkrs((const unsigned char*)Vc + kvoff, nrec);
    const rsrc_t rwo = mkrs(wo + (size_t)(384 * P + 48 * w) * D + 64 * cg, cg < 10 ? 48 * D * 2 - 128 * cg : 0);
    const int voff = r * 384 + c * 16, l48 = lane < 48 ? lane : 47;
    v4u kr[11][3], vr[11][3];
    unsigned tag;
    const unsigned tst = (unsigned)__builtin_amdgcn_s_memrealtime() + TA0;
#define PACE(tt, lo) do { waitvm<(lo) + BW>(); if ((int)((unsigned)__builtin_amdgcn_s_memrealtime() - tst - (tt)) < 0) waitvm<lo>(); } while (0)
#define LDV(t) do { PACE(10 * TAU + 2 * TAU / 3 + (t) * TAU, SP); _Pragma("unroll") for (int j = 0; j < 3; ++j) vr[t][j] = ldnt(rv, voff + j * 128, (t) * TS); SB(); } while (0)
#define LDVN(t) do { PACE(10 * TAU + 2 * TAU / 3 + (t) * TAU, SP2); _Pragma("unroll") for (int j = 0; j < 3; ++j) vr[t][j] = ldnt(rv, voff + j * 128, (t) * TS); SB(); } while (0)
#define VPERM(u) do { _Pragma("unroll") for (int j = 0; j < 3; ++j) pairperm(vr[2 * (u)][j], vr[2 * (u) + 1][j], vp[u][j]); SB(); } while (0)
#define PV(u) do { _Pragma("unroll") for (int j = 0; j < 3; ++j) _Pragma("unroll") for (int k = 0; k < 8; ++k) acc[j * 8 + k] = dot2(vp[u][j][k], pp[u], acc[j * 8 + k]); PIN24(acc); SB(); } while (0)
#pragma unroll
    for (int t = 0; t < 11; ++t) {
      if (t >= 2) PACE(t * TAU, SP);
      if (t == 8) tag = ldsc1(rw, WS_EP);
#pragma unroll
      for (int j = 0; j < 3; ++j) { kr[t][j] = ldnt(rk, voff + j * 128, t * TS); SB(); }
    }
    unsigned vp[5][3][8];
    v4u qg;
#pragma unroll
    for (int t = 0; t <= 6; ++t) {
      if (t == 5) { qg = ldsc4(rw, WS_QG + h * 768 + l48 * 16); SB(); }
      LDV(t);
      if (t == 0) { tag += 1u; asm volatile("" : "+v"(tag)); SB(); }
      if (t == 3) VPERM(0);
      if (t == 5) VPERM(1);
    }
    if (__any(bad4(qg, tag) != 0u)) {
#pragma unroll 1
      for (unsigned sp = 0; sp < SPIN_CAP; ++sp) {
        MEMCLOB();
        qg = ldsc4(rw, WS_QG + h * 768 + l48 * 16);
        if (!__any(bad4(qg, tag) != 0u)) break;
        __builtin_amdgcn_s_sleep(2);
      }
    }
    LAS unsigned* qs = (LAS unsigned*)(L + LDS_QS) + w * 96;
    *(LAS v2u*)(qs + 2 * l48) = v2u{qg.x, qg.z};
    lds_fence();
    v4u qp[3];
#pragma unroll
    for (int j = 0; j < 3; ++j) qp[j] = *(const LAS v4u*)(qs + j * 32 + c * 4);
    float sc[12];
#pragma unroll
    for (int t = 0; t < 11; ++t) {
      float d = 0.f;
#pragma unroll
      for (int j = 0; j < 3; ++j)
#pragma unroll
        for (int k = 0; k < 4; ++k) d = dot2(kr[t][j][k], qp[j][k], d);
      d += dpp<XOR1>(d); d += dpp<XOR2>(d); d += dpp<XOR7>(d);
      d = rbf(rbf(d) * QSCALE);
      sc[t] = (t < 10 || nt == 11) ? d : -INFINITY;
      asm volatile("" : "+v"(sc[t]));
      if (t == 4) { SB(); LDVN(7); }
      if (t == 10) { SB(); LDVN(8); }
    }
    float mw = sc[0];
#pragma unroll
    for (int t = 1; t < 11; ++t) mw = fmaxf(mw, sc[t]);
    mw = fmaxf(mw, dpp<XOR8>(mw)); mw = max16(mw); mw = max32(mw);
    float lw = 0.f;
#pragma unroll
    for (int t = 0; t < 11; ++t) { sc[t] = __builtin_amdgcn_exp2f((sc[t] - mw) * LOG2E); lw += sc[t]; }
    sc[11] = 0.f;
    lw += dpp<XOR8>(lw); lw = fold16(lw, lw); lw = fold32(lw, lw);
    unsigned pp[6];
#pragma unroll
    for (int u = 0; u < 6; ++u) pp[u] = pack2(sc[2 * u], sc[2 * u + 1]);
    ((LAS v2f*)(L + LDS_HB))[w] = v2f{mw, lw};
    float acc[24];
#pragma unroll
    for (int i = 0; i < 24; ++i) acc[i] = 0.f;
    v4u wr[6];
#define LDWO() do { _Pragma("unroll") for (int i = 0; i < 6; ++i) wr[i] = ldnt(rwo, r * (2 * D * 2) + c * 16, (16 * (i >> 1) + (i & 1)) * D * 2); SB(); } while (0)
    float fw;
#define MLPUB() do { \
      lds_barrier(); \
      const v2f ml = ((const LAS v2f*)(L + LDS_HB))[c]; \
      float M = ml.x; M = fmaxf(M, dpp<XOR1>(M)); M = fmaxf(M, dpp<XOR2>(M)); M = fmaxf(M, dpp<XOR7>(M)); \
      const float Lc = sum8(ml.y * __builtin_amdgcn_exp2f((ml.x - M) * LOG2E)); \
      fw = __builtin_amdgcn_exp2f((mw - M) * LOG2E); \
      asm volatile("" : "+v"(fw)); \
      if (w == 7) stsc4(v4u{u32(M), tag, u32(Lc), tag}, rw, moff(lane < 8, WS_MLG + (h * 8 + s) * 128 + lane * 16)); \
      SB(); } while (0)
    const int hh = 2 * P + (w >> 2), mloff = moff(lane < 8, WS_MLG + (hh * 8 + (c < 6 ? c : 5)) * 128);
    const int dp = 48 * (w & 1) + l48, hf = lane >> 5, p24 = (lane & 31) < 24 ? (lane & 31) : 23;
    int a_rw = LDS_RED + (w * 192 + c * 24 + r * 3) * 4, a_rr = LDS_RED + (((dp >> 2) & 7) * 12 + (dp >> 5) * 4 + (dp & 3)) * 8;
    int a_op = moff(lane < 48, WS_OP + (h * 8 + s) * 2048 + dp * 16), opoff = WS_OP + (hh * 8 + 3 * hf) * 2048 + (24 * (w & 3) + p24) * 16;
    int a_ww = LDS_VEC + (w * 64 + c * 8 + r) * 4, a_wp = WS_WP + (2 * w + hf) * WP_REP + (P * D + 64 * cg + 2 * (lane & 31)) * 4;
    asm volatile("" : "+v"(a_rw), "+v"(a_rr), "+v"(a_op), "+v"(opoff), "+v"(a_ww), "+v"(a_wp));
    MLPUB(); LDVN(9); PV(0); PV(1); VPERM(2); PV(2); LDVN(10); VPERM(3); PV(3);
    VPERM(4); PV(4);
    LDWO();
#pragma unroll
    for (int j = 0; j < 3; ++j) fma8(acc, j * 8, lo16(pp[5]), vr[10][j]);
    PIN24(acc); SB();
    {
      float y[12], z[6];
#pragma unroll
      for (int i = 0; i < 12; ++i) y[i] = fold32(acc[i], acc[i + 12]);
#pragma unroll
      for (int i = 0; i < 6; ++i) z[i] = fold16(y[i], y[i + 6]);
      LAS float* red = (LAS float*)(L + a_rw);
#pragma unroll
      for (int i = 0; i < 3; ++i) { const float t0 = z[i] + dpp<XOR8>(z[i]), t1 = z[i + 3] + dpp<XOR8>(z[i + 3]); red[i] = ((lane & 8) ? t1 : t0) * fw; }
    }
    SB();
    lds_barrier();
    if (w < 2) {
      const LAS v2f* red = (const LAS v2f*)(L + a_rr);
      v2f t[8];
#pragma unroll
      for (int i = 0; i < 8; ++i) t[i] = red[i * 96];
      const v2f o = ((t[0] + t[1]) + (t[2] + t[3])) + ((t[4] + t[5]) + (t[6] + t[7]));
      stsc4(v4u{u32(o.x), tag, u32(o.y), tag}, rw, a_op);
    }
    SB();
    v4u ml = ldsc4(rw, mloff);
    if (cg < 10) {
      v4u op[3];
      unsigned wp[3][8];
      if (__any(lane < 8 && bad4(ml, tag) != 0u)) {
#pragma unroll 1
        for (unsigned sp = 0; sp < SPIN_CAP; ++sp) {
          MEMCLOB();
          ml = ldsc4(rw, mloff);
          if (!__any(lane < 8 && bad4(ml, tag) != 0u)) break;
          __builtin_amdgcn_s_sleep(2);
        }
      }
      float wv;
      {
        float M = f32(ml.x); M = fmaxf(M, dpp<XOR1>(M)); M = fmaxf(M, dpp<XOR2>(M)); M = fmaxf(M, dpp<XOR7>(M));
        const float e = c < 6 ? __builtin_amdgcn_exp2f((f32(ml.x) - M) * LOG2E) : 0.f;
        wv = e / sum8(e * f32(ml.z));
        asm volatile("" : "+v"(wv));
      }
#pragma unroll
      for (int i = 0; i < 3; ++i) op[i] = ldsc4(rw, opoff, i * 2048);
      SB();
      float w3[3];
#pragma unroll
      for (int i = 0; i < 3; ++i) w3[i] = f32((unsigned)__builtin_amdgcn_ds_bpermute((3 * hf + i) * 4, (int)u32(wv)));
#pragma unroll
      for (int i = 0; i < 3; ++i) pairperm(wr[2 * i], wr[2 * i + 1], wp[i]);
      const v4u t4 = tag4(tag);
      unsigned tg0 = (lane & 1) ? t4.z : t4.x, tg1 = (lane & 1) ? t4.w : t4.y;
      asm volatile("" : "+v"(w3[0]), "+v"(w3[1]), "+v"(w3[2]), "+v"(tg0), "+v"(tg1));
      if (__any((bad4(op[0], tag) | bad4(op[1], tag) | bad4(op[2], tag)) != 0u)) {
#pragma unroll 1
        for (unsigned sp = 0; sp < SPIN_CAP; ++sp) {
          MEMCLOB();
#pragma unroll
          for (int i = 0; i < 3; ++i) op[i] = ldsc4(rw, opoff, i * 2048);
          if (!__any((bad4(op[0], tag) | bad4(op[1], tag) | bad4(op[2], tag)) != 0u)) break;
          __builtin_amdgcn_s_sleep(1);
        }
      }
      unsigned ov[3];
      {
        float o0 = fmaf(w3[2], f32(op[2].x), fmaf(w3[1], f32(op[1].x), w3[0] * f32(op[0].x))), o1 = fmaf(w3[2], f32(op[2].z), fmaf(w3[1], f32(op[1].z), w3[0] * f32(op[0].z)));
        o0 = fold32(o0, o0); o1 = fold32(o1, o1);
        const int opk = (int)pack2(o0, o1);
#pragma unroll
        for (int i = 0; i < 3; ++i) ov[i] = (unsigned)__builtin_amdgcn_ds_bpermute((8 * i + r) * 4, opk);
      }
      float a[8];
#pragma unroll
      for (int k = 0; k < 8; ++k) a[k] = dot2(wp[2][k], ov[2], dot2(wp[1][k], ov[1], dot2(wp[0][k], ov[0], 0.f)));
      float y[4], z[2];
#pragma unroll
      for (int i = 0; i < 4; ++i) y[i] = fold32(a[i], a[i + 4]);
#pragma unroll
      for (int i = 0; i < 2; ++i) { z[i] = fold16(y[i], y[i + 2]); z[i] += dpp<XOR8>(z[i]); }
      *(LAS float*)(L + a_ww) = (lane & 8) ? z[1] : z[0];
      lds_barrier();
      if (w < 4) {
        const LAS v2f* red = (const LAS v2f*)(L + LDS_VEC) + (lane & 31);
        v2f t[8];
#pragma unroll
        for (int i = 0; i < 8; ++i) t[i] = red[i * 32];
        const v2f o = ((t[0] + t[1]) + (t[2] + t[3])) + ((t[4] + t[5]) + (t[6] + t[7]));
        stsc2(v2u{pk24(o.x, tg0), pk24(o.y, tg1)}, rw, a_wp);
      }
    }
  } else {
    const int b = blockIdx.x, xg = px, v = pk;
    const int t320 = tid < 320 ? tid : 319;
    unsigned tag, cpair;
#define LDC() do { MEMCLOB(); cpair = ((const GAS unsigned*)cvec)[t320]; tag = ldsc1(rw, WS_EP); SB(); } while (0)
    unsigned xpair, xall[5];
    float ln0, ln1;
    v4u u0[10], u1[10], sg[4];
    unsigned p0[5][8], p1[5][8];
    const int rp = lane & 31, hf = lane >> 5;
#define FASTG(p) ((p) + ((p) + 4) / 7)
#define LDX() do { xpair = ((const GAS unsigned*)x)[t320]; _Pragma("unroll") for (int i = 0; i < 5; ++i) xall[i] = ((const GAS unsigned*)x)[lane + 64 * i]; SB(); } while (0)
#define SILU() do { asm volatile("" ::"v"(pf[0]), "v"(pf[1]), "v"(pf[2]), "v"(pf[3])); tag += 1u; const float c0 = lo16(cpair), c1 = hi16(cpair); \
      ((LAS unsigned*)(L + LDS_VEC))[t320] = pack2(c0 / (1.f + expf(-c0)), c1 / (1.f + expf(-c1))); SB(); } while (0)
#define LNX() do { float sm = 0.f; \
      _Pragma("unroll") for (int i = 0; i < 5; ++i) sm += lo16(xall[i]) + hi16(xall[i]); \
      const float mean = wave_sum(sm) * (1.f / D); float sq = 0.f; \
      _Pragma("unroll") for (int i = 0; i < 5; ++i) { const float a = lo16(xall[i]) - mean, bb = hi16(xall[i]) - mean; sq += a * a + bb * bb; } \
      const float var = wave_sum(sq) * (1.f / D); \
      const float mb = rbf(mean), sd = rbf(sqrtf(rbf(rbf(var) + 1e-5f))); \
      ln0 = rbf(rbf(lo16(xpair) - mb) / sd); ln1 = rbf(rbf(hi16(xpair) - mb) / sd); \
      asm volatile("" : "+v"(ln0), "+v"(ln1)); SB(); } while (0)
#define LDSAG() do { _Pragma("unroll") for (int i = 0; i < 4; ++i) sg[i] = ldsc4(rw, moff(tid < 320, WS_SAG + tid * 16), (i & 1) * 10240 + (i >> 1) * 5120); } while (0)
#define MAKEH() do { if (w < 5) { \
      if (__any((bad4(sg[0], tag) | bad4(sg[1], tag) | bad4(sg[2], tag) | bad4(sg[3], tag)) != 0u)) { \
        _Pragma("unroll 1") for (unsigned sp = 0; sp < SPIN_CAP; ++sp) { \
          MEMCLOB(); LDSAG(); \
          if (!__any((bad4(sg[0], tag) | bad4(sg[1], tag) | bad4(sg[2], tag) | bad4(sg[3], tag)) != 0u)) break; \
          __builtin_amdgcn_s_sleep(1); } } \
      const float sa0 = rbf(f32(sg[0].x) + f32(sg[1].x)), sa1 = rbf(f32(sg[0].z) + f32(sg[1].z)); \
      const float ca0 = rbf(f32(sg[2].x) + f32(sg[3].x)), ca1 = rbf(f32(sg[2].z) + f32(sg[3].z)); \
      ((LAS unsigned*)(L + LDS_VEC2))[tid] = pack2(rbf(ln0 * rbf(1.f + ca0)) + sa0, rbf(ln1 * rbf(1.f + ca1)) + sa1); } \
      lds_barrier(); } while (0)
    if (b < 40) {
      const int wi = 5 * xg + v, ga_ = wi < 20 ? wi : wi - 20, ha_ = wi < 20 ? 0 : 1, uu = b - 8;
      const bool two = v == 0 || (uu >= 12 && uu < 18);
      const int g0 = v == 0 ? FASTG(20 + xg) : (uu < 12 ? 8 * uu + 3 : FASTG(uu - 12));
      const int g1 = v == 0 ? FASTG(28 + xg) : FASTG(24 + uu);
      v4u ua[6];
      LDC();
      LDX();
      {
        const rsrc_t ra = mkrs(wmod + (size_t)(320 * ha_ + 40 * w) * (6 * D), 40 * 6 * D * 2);
#pragma unroll
        for (int i = 0; i < 6; ++i) ua[i] = ldnt(ra, 2 * r * (6 * D * 2) + ga_ * 128 + c * 16, (16 * (i >> 1) + (i & 1)) * (6 * D * 2));
        SB();
      }
      SILU();
      lds_barrier();
      unsigned hv[3];
      {
        const LAS unsigned* sc = (const LAS unsigned*)(L + LDS_VEC) + 160 * ha_ + 20 * w + r;
        hv[0] = sc[0]; hv[1] = sc[8]; hv[2] = sc[16]; hv[2] = r < 4 ? hv[2] : 0u;
      }
      unit_load<0, 5, true>(u0, wmod, wq, false, g0, w, lane);
      {
        unsigned pa[3][8];
#pragma unroll
        for (int i = 0; i < 3; ++i) pairperm(ua[2 * i], ua[2 * i + 1], pa[i]);
        float a[8];
#pragma unroll
        for (int k = 0; k < 8; ++k) a[k] = dot2(pa[2][k], hv[2], dot2(pa[1][k], hv[1], dot2(pa[0][k], hv[0], 0.f)));
        float y[4], z[2];
#pragma unroll
        for (int i = 0; i < 4; ++i) y[i] = fold32(a[i], a[i + 4]);
#pragma unroll
        for (int i = 0; i < 2; ++i) z[i] = fold16(y[i], y[i + 2]);
        const float t0 = z[0] + dpp<XOR8>(z[0]), t1 = z[1] + dpp<XOR8>(z[1]);
        ((LAS float*)(L + LDS_RED))[w * 64 + c * 8 + r] = (lane & 8) ? t1 : t0;
        lds_barrier();
        if (w == 0) {
          const LAS v2f* red = (const LAS v2f*)(L + LDS_RED) + (lane & 31);
          v2f sm = red[0];
#pragma unroll
          for (int i = 1; i < 8; ++i) sm += red[i * 32];
          stsc4(v4u{u32(sm.x), tag, u32(sm.y), tag}, rw, moff(lane < 32, WS_SAG + ha_ * 10240 + ga_ * 512 + lane * 16));
        }
      }
      SB();
      unit_load<5, 10, true>(u0, wmod, wq, false, g0, w, lane);
      LNX();
      if (two) unit_load<0, 10, true>(u1, wmod, wq, false, g1, w, lane);
      waitvm<2>();
      LDSAG();
      SB();
      unit_prep(u0, p0);
      if (two) unit_prep(u1, p1);
      SB();
      MAKEH();
      unit_partial(p0, L, LDS_VEC2, LDS_RED + 2048, w, lane);
      if (two) unit_partial(p1, L, LDS_VEC2, LDS_RED + 4096, w, lane);
      lds_barrier();
      if (w == 0 || (w == 1 && two)) unit_publish(L, LDS_RED + 2048 + w * 2048, lane, WS_QG + (w ? g1 : g0) * 256, tag, rw);
      SB();
      unit_load<0, 10, true>(u0, wmod, wq, true, 20 + b, w, lane);
      unit_prep(u0, p0);
      unit_partial(p0, L, LDS_VEC, LDS_RED, w, lane);
      lds_barrier();
      if (w == 0) unit_publish(L, LDS_RED, lane, WS_MODG + (20 + b) * 256, tag, rw);
    } else {
      const int f = 3 * px + pk - 5, fi = f - ((f + 4) >> 3);
      const int g0 = FASTG(42 + fi), g1 = FASTG(63 + fi);
      LDC();
      LDX();
      if ((f & 7) != 3) {
        unit_load<0, 6, false>(u0, wmod, wq, false, g0, w, lane);
        asm volatile("" : "+v"(tag), "+v"(xpair) : "v"(pf[0]), "v"(pf[1]), "v"(pf[2]), "v"(pf[3]), "v"(cpair));
        tag += 1u;
        unit_load<6, 10, true>(u0, wmod, wq, false, g0, w, lane);
        LNX();
        unit_load<0, 8, true>(u1, wmod, wq, false, g1, w, lane);
        unit_prep(u0, p0);
        unit_load<8, 10, true>(u1, wmod, wq, false, g1, w, lane);
        waitvm<2>();
        LDSAG();
        SB();
        unit_prep(u1, p1);
        SB();
        MAKEH();
        unit_partial(p0, L, LDS_VEC2, LDS_RED + 2048, w, lane);
        unit_partial(p1, L, LDS_VEC2, LDS_RED + 4096, w, lane);
        lds_barrier();
        if (w < 2) unit_publish(L, LDS_RED + 2048 + w * 2048, lane, WS_QG + (w ? g1 : g0) * 256, tag, rw);
        SB();
      } else {
        asm volatile("; no front work" : "+v"(tag), "+v"(xpair) : "v"(pf[0]), "v"(pf[1]), "v"(pf[2]), "v"(pf[3]), "v"(cpair));
        tag += 1u;
      }
      SB();
      const rsrc_t rd = mkrs(wd + (size_t)(f * 64 + 8 * w) * D, 8 * D * 2);
      const rsrc_t rg = mkrs(wg + f * 64, D * FF * 2 - f * 128), ru = mkrs(wu + f * 64, D * FF * 2 - f * 128);
      const int gvo = (40 * w + r) * (FF * 2) + c * 16;
      const int g4 = lane >> 4, q4 = (lane >> 2) & 3, p4 = lane & 3;
      v4u gu[20], dw[5][2];
#define LDD(nn, lo, hi) do { _Pragma("unroll") for (int i = lo; i < hi; ++i) { waitvm<CP>(); nn[i] = ldnt(rd, lane * 16, i * 1024); SB(); } } while (0)
#define LDGU(nn, p, lo, hi) do { _Pragma("unroll") for (int i = lo; i < hi; ++i) { waitvm<CP>(); nn[i] = ldnt(i < 5 ? rg : ru, gvo, (320 * (p) + 8 * (i % 5)) * FF * 2); SB(); } } while (0)
#define GUROUND(p, nn) do { \
        _Pragma("unroll") for (int i = 0; i < 5; ++i) { \
          const int row = 40 * w + 8 * i + r, a = row * 128 + ((c ^ ((row >> 1) & 7)) << 4); \
          *(LAS v4u*)(L + LDS_BIG + a) = nn[i]; *(LAS v4u*)(L + LDS_BIG + 40960 + a) = nn[5 + i]; } \
        lds_barrier(); \
        { const int row0 = 8 * g4 + q4, row1 = row0 + 4; \
          const LAS unsigned char* q0 = L + LDS_BIG + (p4 >> 1) * 40960 + row0 * 128 + ((w ^ ((row0 >> 1) & 7)) << 4) + (p4 & 1) * 8; \
          const LAS unsigned char* q1 = L + LDS_BIG + (p4 >> 1) * 40960 + row1 * 128 + ((w ^ ((row1 >> 1) & 7)) << 4) + (p4 & 1) * 8; \
          _Pragma("unroll") for (int ks = 0; ks < 10; ++ks) gu[(p) * 10 + ks] = trfrag(q0 + ks * 4096, q1 + ks * 4096); } \
        SB(); } while (0)
      LDGU(u0, 0, 0, 10);
      LDGU(u1, 1, 0, 6);
      GUROUND(0, u0);
      LDGU(u1, 1, 6, 10);
      LDD(u0, 0, 6);
      lds_barrier();
      GUROUND(1, u1);
      LDD(u0, 6, 10);
      lds_barrier();
#pragma unroll
      for (int i = 0; i < 10; ++i) *(LAS v4u*)(L + LDS_BIG + (10 * w + i) * 1024 + lane * 16) = u0[i];
      lds_barrier();
      {
        const LAS unsigned char* q0 = L + LDS_BIG + (8 * g4 + q4) * (D * 2) + w * 160 + p4 * 8;
#pragma unroll
        for (int T = 0; T < 5; ++T)
#pragma unroll
          for (int ks = 0; ks < 2; ++ks) dw[T][ks] = trfrag(q0 + ks * 32 * D * 2 + T * 32, q0 + (ks * 32 + 4) * D * 2 + T * 32);
        SB();
      }
      const bool red_on = (w == 5 || w == 6) && f < 20;
      const int opr = 16 * f + 8 * (w - 5) + c;
      int dpo0 = WS_DP + (f * D + 80 * w + lane) * 8, dpo1 = moff(lane < 16, dpo0 + 512), dpr = WS_DP + r * (D * 8) + opr * 16;
      asm volatile("" : "+v"(dpo0), "+v"(dpo1), "+v"(dpr));
      LAS v2f* stat = (LAS v2f*)(L + LDS_QS + 1024);
      float x20 = 0.f, x21 = 0.f;
      v2u ma, mb2, mc;
      const int pi = (tid & ~63) + 2 * rp + hf;
      xpair = (unsigned)__builtin_amdgcn_ds_bpermute((2 * rp + hf) * 4, (int)xpair);
      if (w >= 5) {
        stat[w] = v2f{0.f, 0.f};
        if (red_on) {
#pragma unroll 1
          for (unsigned sp = 0; sp < SPIN_CAP; ++sp) {
            MEMCLOB();
            ma = ldsc2(rw, WS_MODG + (1600 + opr) * 8);
            if (!__any(ma.y != tag)) break;
            __builtin_amdgcn_s_sleep(4);
          }
        }
      } else {
        v4u pv[8];
        unsigned miss = 0x1ffu;
        const int wpoff = WS_WP + px * WP_REP + (hf * D + 128 * w + 4 * rp) * 4;
        const v4u t4 = tag4(tag);
#pragma unroll 1
        for (unsigned sp = 0; sp < SPIN_CAP && miss; ++sp) {
          MEMCLOB();
#pragma unroll
          for (int g = 0; g < 8; ++g) if (__builtin_expect(miss & (1u << g), 0)) pv[g] = ldsc4(rw, wpoff, (2 * g) * D * 4);
          if (__builtin_expect(miss & 256u, 0)) { ma = ldsc2(rw, WS_MODG + (640 + pi) * 8); mb2 = ldsc2(rw, WS_MODG + (960 + pi) * 8); mc = ldsc2(rw, WS_MODG + (1280 + pi) * 8); }
#pragma unroll
          for (int g = 0; g < 8; ++g) if (__builtin_expect(miss & (1u << g), 0)) {
            const v4u e = pv[g] ^ t4;
            if (!__any(((e.x | e.y | e.z | e.w) & 0xffu) != 0u)) {
              miss &= ~(1u << g);
              pv[g].x = u32(fold32(f32(e.x), f32(e.z))); pv[g].y = u32(fold32(f32(e.y), f32(e.w)));
            }
          }
          if (__builtin_expect(miss & 256u, 0)) {
            if (!__any(((ma.y ^ tag) | (mb2.y ^ tag) | (mc.y ^ tag)) != 0u)) { miss &= 255u; mc.x = pack2(1.f + lo16(mc.x), 1.f + hi16(mc.x)); }
          }
        }
        const v2f at = {((f32(pv[0].x) + f32(pv[1].x)) + (f32(pv[2].x) + f32(pv[3].x))) + ((f32(pv[4].x) + f32(pv[5].x)) + (f32(pv[6].x) + f32(pv[7].x))),
                        ((f32(pv[0].y) + f32(pv[1].y)) + (f32(pv[2].y) + f32(pv[3].y))) + ((f32(pv[4].y) + f32(pv[5].y)) + (f32(pv[6].y) + f32(pv[7].y)))};
        x20 = rbf(lo16(xpair) + rbf(lo16(ma.x) * rbf(at.x))); x21 = rbf(hi16(xpair) + rbf(hi16(ma.x) * rbf(at.y)));
        *(LAS v2f*)(L + LDS_VEC2 + pi * 8) = v2f{x20, x21};
        const float piv = __builtin_bit_cast(float, __builtin_amdgcn_readfirstlane(__builtin_bit_cast(int, x20)));
        const float d0 = x20 - piv, d1 = x21 - piv;
        const float s1 = wave_sum(d0 + d1), s2 = wave_sum(d0 * d0 + d1 * d1);
        stat[w] = v2f{piv + s1 * (1.f / 128), s2 - s1 * s1 * (1.f / 128)};
      }
      lds_barrier();
      if (w < 5) {
        const v2f st = stat[c];
        const float mean = sum8(st.x) * 0.2f, dm = st.x - mean;
        const float m2 = sum8(c < 5 ? fmaf(128.f * dm, dm, st.y) : 0.f);
        const float mb = rbf(mean), sd = rbf(__builtin_amdgcn_sqrtf(rbf(rbf(fmaxf(m2, 0.f) * (1.f / D)) + 1e-5f)));
        const float rs = __builtin_amdgcn_rcpf(sd);
        const float l0 = rbf(rbf(x20 - mb) * rs), l1 = rbf(rbf(x21 - mb) * rs);
        ((LAS unsigned*)(L + LDS_HB))[pi] = pack2(rbf(l0 * lo16(mc.x)) + lo16(mb2.x), rbf(l1 * hi16(mc.x)) + hi16(mb2.x));
      }
      lds_barrier();
      {
        const LAS v4u* hb = (const LAS v4u*)(L + LDS_HB) + g4;
        v4u ha[20];
#pragma unroll
        for (int ks = 0; ks < 20; ++ks) ha[ks] = hb[4 * ks];
        SB();
        v4f acc = {0.f, 0.f, 0.f, 0.f};
#pragma unroll
        for (int ks = 0; ks < 20; ++ks) acc = mfma16(ha[ks], gu[ks], acc);
        const float vv = rbf(acc.x);
        ((LAS __bf16*)(L + LDS_QS))[8 * w + c] = (__bf16)(vv * dpp<XOR8>(vv));
      }
      lds_barrier();
      {
        const LAS v4u* hp2 = (const LAS v4u*)(L + LDS_QS) + g4;
        const v4u h0 = hp2[0], h1 = hp2[4];
        v4f acc[5];
#pragma unroll
        for (int T = 0; T < 5; ++T) acc[T] = mfma16(h0, dw[T][0], v4f{0.f, 0.f, 0.f, 0.f});
#pragma unroll
        for (int T = 0; T < 5; ++T) acc[T] = mfma16(h1, dw[T][1], acc[T]);
        const float z = (lane & 32) ? ((lane & 16) ? acc[3].x : acc[2].x) : ((lane & 16) ? acc[1].x : acc[0].x);
        stsc2(v2u{u32(z), tag}, rw, dpo0);
        stsc2(v2u{u32(acc[4].x), tag}, rw, dpo1);
      }
      if (red_on) {
        const v2f x2v = ((const LAS v2f*)(L + LDS_VEC2))[opr];
        v4u dv[3];
#pragma unroll 1
        for (unsigned sp = 0; sp < SPIN_CAP; ++sp) {
          MEMCLOB();
#pragma unroll
          for (int i = 0; i < 3; ++i) dv[i] = __builtin_amdgcn_raw_buffer_load_b128(rw, dpr, i * 8 * D * 8, AUX_SC1);
          if (!__any((bad4(dv[0], tag) | bad4(dv[1], tag) | bad4(dv[2], tag)) != 0u)) break;
        }
        float z0 = (f32(dv[0].x) + f32(dv[1].x)) + f32(dv[2].x), z1 = (f32(dv[0].z) + f32(dv[1].z)) + f32(dv[2].z);
        z0 += dpp<XOR8>(z0); z1 += dpp<XOR8>(z1);
        z0 = fold16(z0, z0); z1 = fold16(z1, z1); z0 = fold32(z0, z0); z1 = fold32(z1, z1);
        const unsigned o = pack2(x2v.x + rbf(lo16(ma.x) * rbf(z0)), x2v.y + rbf(hi16(ma.x) * rbf(z1)));
        __builtin_amdgcn_raw_buffer_store_b32(o, mkrs(out, D * 2), moff(lane < 8, opr * 4), 0, 0);
        if (f == 0 && w == 5) stsc1(tag, rw, moff(lane == 0, WS_EP));
      }
    }
  }
}

extern "C" void kernel_launch(void* const* d_in, const int* in_sizes, int n_in, void* d_out, int out_size, void* d_ws, size_t ws_size,
                              hipStream_t stream) {
  hipLaunchKernelGGL(mega, dim3(256), dim3(512), 0, stream, (const u16*)d_in[0], (const u16*)d_in[1], (const u16*)d_in[2], (const u16*)d_in[3],
                     (const u16*)d_in[4], (const u16*)d_in[6], (const u16*)d_in[7], (const u16*)d_in[8], (const u16*)d_in[9], (const u16*)d_in[10],
                     (u16*)d_out, (unsigned char*)d_ws);
}
```

```cpp
#include <hip/hip_runtime.h>
#include <hip/hip_bf16.h>
#include <cstdint>

typedef unsigned short u16;
typedef unsigned v4u __attribute__((ext_vector_type(4)));
typedef unsigned v2u __attribute__((ext_vector_type(2)));
typedef float v4f __attribute__((ext_vector_type(4)));
typedef float v2f __attribute__((ext_vector_type(2)));
typedef __bf16 bf16x2 __attribute__((ext_vector_type(2)));
typedef short v4s __attribute__((ext_vector_type(4)));
typedef __bf16 bf16x8 __attribute__((ext_vector_type(8)));
typedef __amdgpu_buffer_rsrc_t rsrc_t;
#define LAS __attribute__((address_space(3)))
#define GAS __attribute__((address_space(1)))
#define DEV __device__ __forceinline__

#define PFKB 24
constexpr int CP = 5;
constexpr int SP = 2, SP2 = 5, BW = 3;
constexpr int TAU = 76, TA0 = -30;

constexpr int D = 640, SK = 4096, DH = 192, FF = 1536;
constexpr int AUX_NT = 2, AUX_SC1 = 16;
constexpr int WS_EP = 0, WS_MODG = 4096, WS_QG = 32768, WS_MLG = 65536, WS_OP = 131072, WS_WP = 2 << 20, WP_REP = 40960, WS_SAG = 98304, WS_DP = 786432, WS_BYTES = 2 << 20;
constexpr unsigned SPIN_CAP = 1u << 17;
constexpr float LOG2E = 1.4426950408889634f;
constexpr float QSCALE = 0.07216878364870322f;

constexpr int LDS_RED = 0;
constexpr int LDS_VEC = 6144;
constexpr int LDS_VEC2 = 8704;
constexpr int LDS_HB = 11264;
constexpr int LDS_QS = 12800;
constexpr int LDS_BIG = 16896;
constexpr int LDS_BYTES = 16896 + 81920;

DEV float lo16(unsigned w) { return __uint_as_float(w << 16); }
DEV float hi16(unsigned w) { return __uint_as_float(w & 0xffff0000u); }
DEV float f32(unsigned w) { return __uint_as_float(w); }
DEV unsigned u32(float f) { return __float_as_uint(f); }
DEV float rbf(float f) { return (float)(__bf16)f; }
DEV unsigned pack2(float a, float b) { bf16x2 v = {(__bf16)a, (__bf16)b}; return __builtin_bit_cast(unsigned, v); }
DEV float dot2(unsigned a, unsigned b, float acc) { return __builtin_amdgcn_fdot2_f32_bf16(__builtin_bit_cast(bf16x2, a), __builtin_bit_cast(bf16x2, b), acc, false); }
template <int CTRL> DEV float dpp(float x) { return __builtin_bit_cast(float, __builtin_amdgcn_mov_dpp(__builtin_bit_cast(int, x), CTRL, 0xf, 0xf, true)); }
constexpr int XOR1 = 0xB1, XOR2 = 0x4E, XOR7 = 0x141, XOR8 = 0x128;
DEV float fold32(float a, float b) { auto s = __builtin_amdgcn_permlane32_swap(u32(a), u32(b), false, false); return f32(s[0]) + f32(s[1]); }
DEV float fold16(float a, float b) { auto s = __builtin_amdgcn_permlane16_swap(u32(a), u32(b), false, false); return f32(s[0]) + f32(s[1]); }
DEV float max32(float a) { auto s = __builtin_amdgcn_permlane32_swap(u32(a), u32(a), false, false); return fmaxf(f32(s[0]), f32(s[1])); }
DEV float max16(float a) { auto s = __builtin_amdgcn_permlane16_swap(u32(a), u32(a), false, false); return fmaxf(f32(s[0]), f32(s[1])); }
DEV float wave_sum(float x) { x += dpp<XOR1>(x); x += dpp<XOR2>(x); x += dpp<XOR7>(x); x += dpp<XOR8>(x); x = fold16(x, x); return fold32(x, x); }
DEV rsrc_t mkrs(const void* p, int bytes) {
  const unsigned long long a = (unsigned long long)p;
  const unsigned long long u = ((unsigned long long)(unsigned)__builtin_amdgcn_readfirstlane((int)(a >> 32)) << 32) | (unsigned)__builtin_amdgcn_readfirstlane((int)a);
  return __builtin_amdgcn_make_buffer_rsrc((void*)u, 0, __builtin_amdgcn_readfirstlane(bytes), 0x00020000);
}
DEV v4u ldnt(rsrc_t rs, int voff, int soff) { return __builtin_amdgcn_raw_buffer_load_b128(rs, voff, soff, AUX_NT); }
#define MEMCLOB() asm volatile("" ::: "memory")
DEV v4u ldsc4(rsrc_t rs, int voff, int soff = 0) { return __builtin_amdgcn_raw_buffer_load_b128(rs, voff, soff, AUX_SC1); }
DEV v2u ldsc2(rsrc_t rs, int voff, int soff = 0) { return __builtin_amdgcn_raw_buffer_load_b64(rs, voff, soff, AUX_SC1); }
DEV unsigned ldsc1(rsrc_t rs, int voff) { return __builtin_amdgcn_raw_buffer_load_b32(rs, voff, 0, AUX_SC1); }
DEV void stsc4(v4u v, rsrc_t rs, int voff) { __builtin_amdgcn_raw_buffer_store_b128(v, rs, voff, 0, AUX_SC1); }
DEV void stsc2(v2u v, rsrc_t rs, int voff) { __builtin_amdgcn_raw_buffer_store_b64(v, rs, voff, 0, AUX_SC1); }
DEV void stsc1(unsigned v, rsrc_t rs, int voff) { __builtin_amdgcn_raw_buffer_store_b32(v, rs, voff, 0, AUX_SC1); }
DEV int moff(bool on, int off) { return on ? off : 0x40000000; }
DEV v4u tag4(unsigned tag) { return v4u{tag & 0xffu, (tag >> 8) & 0xffu, (tag + (tag >> 16)) & 0xffu, tag >> 24}; }
DEV unsigned pk24(float f, unsigned t8) { const unsigned u = u32(f); return ((u + 0x7fu + ((u >> 8) & 1u)) & 0xffffff00u) | t8; }
DEV unsigned bad4(v4u v, unsigned tag) { return (v.y ^ tag) | (v.w ^ tag); }
#define PIN24(a) asm volatile("" : "+v"(a[0]), "+v"(a[1]), "+v"(a[2]), "+v"(a[3]), "+v"(a[4]), "+v"(a[5]), "+v"(a[6]), "+v"(a[7]), "+v"(a[8]), "+v"(a[9]), "+v"(a[10]), "+v"(a[11]), \
  "+v"(a[12]), "+v"(a[13]), "+v"(a[14]), "+v"(a[15]), "+v"(a[16]), "+v"(a[17]), "+v"(a[18]), "+v"(a[19]), "+v"(a[20]), "+v"(a[21]), "+v"(a[22]), "+v"(a[23]))
template <int N> DEV void waitvm() { asm volatile("s_waitcnt vmcnt(%0)" ::"i"(N) : "memory"); }
#define SB() __builtin_amdgcn_sched_barrier(0)
DEV void lds_barrier() { asm volatile("s_waitcnt lgkmcnt(0)" ::: "memory"); __builtin_amdgcn_s_barrier(); asm volatile("" ::: "memory"); }
DEV void lds_fence() { asm volatile("s_waitcnt lgkmcnt(0)" ::: "memory"); }

DEV float sum8(float x) { x += dpp<XOR1>(x); x += dpp<XOR2>(x); x += dpp<XOR7>(x); return x; }
DEV v4u trfrag(const LAS unsigned char* p0, const LAS unsigned char* p1) {
  const v2u a = __builtin_bit_cast(v2u, __builtin_amdgcn_ds_read_tr16_b64_v4i16((LAS v4s*)p0)), b = __builtin_bit_cast(v2u, __builtin_amdgcn_ds_read_tr16_b64_v4i16((LAS v4s*)p1));
  v4u o = {a.x, a.y, b.x, b.y};
  asm volatile("" : "+v"(o));
  return o;
}
DEV v4f mfma16(v4u a, v4u b, v4f c) { return __builtin_amdgcn_mfma_f32_16x16x32_bf16(__builtin_bit_cast(bf16x8, a), __builtin_bit_cast(bf16x8, b), c, 0, 0, 0); }
DEV void pairperm(v4u e, v4u o, unsigned (&p)[8]) {
#pragma unroll
  for (int k = 0; k < 4; ++k) { p[2 * k] = __builtin_amdgcn_perm(o[k], e[k], 0x05040100u); p[2 * k + 1] = __builtin_amdgcn_perm(o[k], e[k], 0x07060302u); }
  asm volatile("" : "+v"(p[0]), "+v"(p[1]), "+v"(p[2]), "+v"(p[3]), "+v"(p[4]), "+v"(p[5]), "+v"(p[6]), "+v"(p[7]));
}
DEV void fma8(float (&acc)[24], int base, float p, v4u v) {
#pragma unroll
  for (int k = 0; k < 4; ++k) { acc[base + 2 * k] = fmaf(p, lo16(v[k]), acc[base + 2 * k]); acc[base + 2 * k + 1] = fmaf(p, hi16(v[k]), acc[base + 2 * k + 1]); }
}
DEV void unit_prep(const v4u (&raw)[10], unsigned (&p)[5][8]) {
#pragma unroll
  for (int i = 0; i < 5; ++i) pairperm(raw[2 * i], raw[2 * i + 1], p[i]);
}
DEV void unit_partial(const unsigned (&p)[5][8], LAS unsigned char* L, int vecoff, int redoff, int w, int lane) {
  const LAS unsigned* sc = (const LAS unsigned*)(L + vecoff) + 40 * w + (lane >> 3);
  float a[8];
#pragma unroll
  for (int k = 0; k < 8; ++k) a[k] = 0.f;
#pragma unroll
  for (int i = 0; i < 5; ++i) { const unsigned hv = sc[8 * i];
#pragma unroll
    for (int k = 0; k < 8; ++k) a[k] = dot2(p[i][k], hv, a[k]); }
  float y[4], z[2];
#pragma unroll
  for (int i = 0; i < 4; ++i) y[i] = fold32(a[i], a[i + 4]);
#pragma unroll
  for (int i = 0; i < 2; ++i) z[i] = fold16(y[i], y[i + 2]);
  const float t0 = z[0] + dpp<XOR8>(z[0]), t1 = z[1] + dpp<XOR8>(z[1]);
  ((LAS float*)(L + redoff))[w * 64 + (lane & 7) * 8 + (lane >> 3)] = (lane & 8) ? t1 : t0;
}
DEV v4u ldp(rsrc_t rs, int voff, int soff) { waitvm<CP>(); const v4u v = ldnt(rs, voff, soff); SB(); return v; }
DEV void unit_publish(LAS unsigned char* L, int redoff, int lane, int outoff, unsigned tag, rsrc_t rw) {
  const LAS v2f* red = (const LAS v2f*)(L + redoff) + (lane & 31);
  v2f s = red[0];
#pragma unroll
  for (int i = 1; i < 8; ++i) s += red[i * 32];
  stsc2(v2u{pack2(s.x, s.y), tag}, rw, moff(lane < 32, outoff + lane * 8));
}
template <int LO, int HI, bool PACED> DEV void unit_load(v4u (&raw)[10], const u16* wmod, const u16* wq, bool is_mod, int g, int w, int lane) {
  const int stride = is_mod ? 6 * D * 2 : 32 * DH * 2;
  const rsrc_t rs = mkrs((const unsigned char*)(is_mod ? wmod : wq) + (size_t)80 * w * stride, 80 * stride);
  const int vo = (lane >> 3) * 2 * stride + g * 128 + (lane & 7) * 16;
#pragma unroll
  for (int i = LO; i < HI; ++i) raw[i] = (PACED ? ldp(rs, vo, (16 * (i >> 1) + (i & 1)) * stride) : ldnt(rs, vo, (16 * (i >> 1) + (i & 1)) * stride));
  SB();
}

__global__ void __launch_bounds__(512, 2)
mega(const u16* x, const u16* Kc, const u16* Vc, const u16* wq, const u16* wo,
     const u16* wg, const u16* wu, const u16* wd, const u16* cvec, const u16* wmod,
     u16* out, unsigned char* ws) {
  __shared__ __attribute__((aligned(16))) unsigned char smem[LDS_BYTES];
  LAS unsigned char* L = (LAS unsigned char*)smem;
  const int px = blockIdx.x & 7, pk = blockIdx.x >> 3;
  const int tid = threadIdx.x, lane = tid & 63, w = __builtin_amdgcn_readfirstlane(tid >> 6);
  const int r = lane >> 3, c = lane & 7;
  v4u pf[4];
  if (pk < 8) {
    const rsrc_t rc = mkrs((const void*)(__builtin_amdgcn_s_getpc() & ~127ull), PFKB * 1024);
#pragma unroll
    for (int i = 0; i < 4; ++i) pf[i] = __builtin_amdgcn_raw_buffer_load_b128(rc, tid * 16, i * 8192, 0);
    MEMCLOB();
  }
  const int ph_ = pk >= 20 ? 1 : 0, P = 2 * px + ph_, cg = pk - 8 - 12 * ph_, hq = cg >= 6 ? 1 : 0, s = cg - 6 * hq, h = 2 * P + hq;
  const int rem = s < 2 ? 6 : 5, t0cu = 85 * s + (s < 2 ? s : 2);
  const int nt = w < rem ? 11 : 10, TS = 8 * 3072;
  int nrec = (80 + rem - w) * 3072;
  unsigned kvoff = (unsigned)(h * 512 + t0cu + w) * 3072u;
  int rot = h % 10;
  asm volatile("" : "+s"(nrec), "+s"(kvoff), "+s"(rot));
#define TOFF(t) ((t) == 10 ? 10 * TS : ((t) + rot >= 10 ? (t) + rot - 10 : (t) + rot) * TS)
  asm volatile("" ::"s"(x), "s"(Kc), "s"(Vc), "s"(wq), "s"(wo), "s"(wg), "s"(wu), "s"(wd), "s"(cvec), "s"(wmod), "s"(out), "s"(ws));
  const rsrc_t rw = mkrs(ws, WS_BYTES);
  if (__builtin_expect(pk >= 8, 1)) {
    const rsrc_t rk = mkrs((const unsigned char*)Kc + kvoff, nrec), rv = mkrs((const unsigned char*)Vc + kvoff, nrec);
    const rsrc_t rwo = mkrs(wo + (size_t)(384 * P + 48 * w) * D + 64 * cg, cg < 10 ? 48 * D * 2 - 128 * cg : 0);
    const int voff = r * 384 + c * 16, l48 = lane < 48 ? lane : 47;
    v4u kr[11][3], vr[11][3];
    unsigned tag;
    const unsigned tst = (unsigned)__builtin_amdgcn_s_memrealtime() + TA0;
#define PACE(tt, lo) do { waitvm<(lo) + BW>(); if ((int)((unsigned)__builtin_amdgcn_s_memrealtime() - tst - (tt)) < 0) waitvm<lo>(); } while (0)
#define LDV(t) do { PACE(10 * TAU + 2 * TAU / 3 + (t) * TAU, SP); _Pragma("unroll") for (int j = 0; j < 3; ++j) vr[t][j] = ldnt(rv, voff + j * 128, TOFF(t)); SB(); } while (0)
#define LDVN(t) do { PACE(10 * TAU + 2 * TAU / 3 + (t) * TAU, SP2); _Pragma("unroll") for (int j = 0; j < 3; ++j) vr[t][j] = ldnt(rv, voff + j * 128, TOFF(t)); SB(); } while (0)
#define VPERM(u) do { _Pragma("unroll") for (int j = 0; j < 3; ++j) pairperm(vr[2 * (u)][j], vr[2 * (u) + 1][j], vp[u][j]); SB(); } while (0)
#define PV(u) do { _Pragma("unroll") for (int j = 0; j < 3; ++j) _Pragma("unroll") for (int k = 0; k < 8; ++k) acc[j * 8 + k] = dot2(vp[u][j][k], pp[u], acc[j * 8 + k]); PIN24(acc); SB(); } while (0)
#pragma unroll
    for (int t = 0; t < 11; ++t) {
      if (t >= 2) PACE(t * TAU, SP);
      if (t == 8) tag = ldsc1(rw, WS_EP);
#pragma unroll
      for (int j = 0; j < 3; ++j) { kr[t][j] = ldnt(rk, voff + j * 128, TOFF(t)); SB(); }
    }
    unsigned vp[5][3][8];
    v4u qg;
#pragma unroll
    for (int t = 0; t <= 6; ++t) {
      if (t == 5) { qg = ldsc4(rw, WS_QG + h * 768 + l48 * 16); SB(); }
      LDV(t);
      if (t == 0) { tag += 1u; asm volatile("" : "+v"(tag)); SB(); }
      if (t == 3) VPERM(0);
      if (t == 5) VPERM(1);
    }
    if (__any(bad4(qg, tag) != 0u)) {
#pragma unroll 1
      for (unsigned sp = 0; sp < SPIN_CAP; ++sp) {
        MEMCLOB();
        qg = ldsc4(rw, WS_QG + h * 768 + l48 * 16);
        if (!__any(bad4(qg, tag) != 0u)) break;
        __builtin_amdgcn_s_sleep(2);
      }
    }
    LAS unsigned* qs = (LAS unsigned*)(L + LDS_QS) + w * 96;
    *(LAS v2u*)(qs + 2 * l48) = v2u{qg.x, qg.z};
    lds_fence();
    v4u qp[3];
#pragma unroll
    for (int j = 0; j < 3; ++j) qp[j] = *(const LAS v4u*)(qs + j * 32 + c * 4);
    float sc[12];
#pragma unroll
    for (int t = 0; t < 11; ++t) {
      float d = 0.f;
#pragma unroll
      for (int j = 0; j < 3; ++j)
#pragma unroll
        for (int k = 0; k < 4; ++k) d = dot2(kr[t][j][k], qp[j][k], d);
      d += dpp<XOR1>(d); d += dpp<XOR2>(d); d += dpp<XOR7>(d);
      d = rbf(rbf(d) * QSCALE);
      sc[t] = (t < 10 || nt == 11) ? d : -INFINITY;
      asm volatile("" : "+v"(sc[t]));
      if (t == 4) { SB(); LDVN(7); }
      if (t == 10) { SB(); LDVN(8); }
    }
    float mw = sc[0];
#pragma unroll
    for (int t = 1; t < 11; ++t) mw = fmaxf(mw, sc[t]);
    mw = fmaxf(mw, dpp<XOR8>(mw)); mw = max16(mw); mw = max32(mw);
    float lw = 0.f;
#pragma unroll
    for (int t = 0; t < 11; ++t) { sc[t] = __builtin_amdgcn_exp2f((sc[t] - mw) * LOG2E); lw += sc[t]; }
    sc[11] = 0.f;
    lw += dpp<XOR8>(lw); lw = fold16(lw, lw); lw = fold32(lw, lw);
    unsigned pp[6];
#pragma unroll
    for (int u = 0; u < 6; ++u) pp[u] = pack2(sc[2 * u], sc[2 * u + 1]);
    ((LAS v2f*)(L + LDS_HB))[w] = v2f{mw, lw};
    float acc[24];
#pragma unroll
    for (int i = 0; i < 24; ++i) acc[i] = 0.f;
    v4u wr[6];
#define LDWO() do { _Pragma("unroll") for (int i = 0; i < 6; ++i) wr[i] = ldnt(rwo, r * (2 * D * 2) + c * 16, (16 * (i >> 1) + (i & 1)) * D * 2); SB(); } while (0)
    float fw;
#define MLPUB() do { \
      lds_barrier(); \
      const v2f ml = ((const LAS v2f*)(L + LDS_HB))[c]; \
      float M = ml.x; M = fmaxf(M, dpp<XOR1>(M)); M = fmaxf(M, dpp<XOR2>(M)); M = fmaxf(M, dpp<XOR7>(M)); \
      const float Lc = sum8(ml.y * __builtin_amdgcn_exp2f((ml.x - M) * LOG2E)); \
      fw = __builtin_amdgcn_exp2f((mw - M) * LOG2E); \
      asm volatile("" : "+v"(fw)); \
      if (w == 7) stsc4(v4u{u32(M), tag, u32(Lc), tag}, rw, moff(lane < 8, WS_MLG + (h * 8 + s) * 128 + lane * 16)); \
      SB(); } while (0)
    const int hh = 2 * P + (w >> 2), mloff = moff(lane < 8, WS_MLG + (hh * 8 + (c < 6 ? c : 5)) * 128);
    const int dp = 48 * (w & 1) + l48, hf = lane >> 5, p24 = (lane & 31) < 24 ? (lane & 31) : 23;
    int a_rw = LDS_RED + (w * 192 + c * 24 + r * 3) * 4, a_rr = LDS_RED + (((dp >> 2) & 7) * 12 + (dp >> 5) * 4 + (dp & 3)) * 8;
    int a_op = moff(lane < 48, WS_OP + (h * 8 + s) * 2048 + dp * 16), opoff = WS_OP + (hh * 8 + 3 * hf) * 2048 + (24 * (w & 3) + p24) * 16;
    int a_ww = LDS_VEC + (w * 64 + c * 8 + r) * 4, a_wp = WS_WP + (2 * w + hf) * WP_REP + (P * D + 64 * cg + 2 * (lane & 31)) * 4;
    asm volatile("" : "+v"(a_rw), "+v"(a_rr), "+v"(a_op), "+v"(opoff), "+v"(a_ww), "+v"(a_wp));
    MLPUB(); LDVN(9); PV(0); PV(1); VPERM(2); PV(2); LDVN(10); VPERM(3); PV(3);
    VPERM(4); PV(4);
    LDWO();
#pragma unroll
    for (int j = 0; j < 3; ++j) fma8(acc, j * 8, lo16(pp[5]), vr[10][j]);
    PIN24(acc); SB();
    {
      float y[12], z[6];
#pragma unroll
      for (int i = 0; i < 12; ++i) y[i] = fold32(acc[i], acc[i + 12]);
#pragma unroll
      for (int i = 0; i < 6; ++i) z[i] = fold16(y[i], y[i + 6]);
      LAS float* red = (LAS float*)(L + a_rw);
#pragma unroll
      for (int i = 0; i < 3; ++i) { const float t0 = z[i] + dpp<XOR8>(z[i]), t1 = z[i + 3] + dpp<XOR8>(z[i + 3]); red[i] = ((lane & 8) ? t1 : t0) * fw; }
    }
    SB();
    lds_barrier();
    if (w < 2) {
      const LAS v2f* red = (const LAS v2f*)(L + a_rr);
      v2f t[8];
#pragma unroll
      for (int i = 0; i < 8; ++i) t[i] = red[i * 96];
      const v2f o = ((t[0] + t[1]) + (t[2] + t[3])) + ((t[4] + t[5]) + (t[6] + t[7]));
      stsc4(v4u{u32(o.x), tag, u32(o.y), tag}, rw, a_op);
    }
    SB();
    v4u ml = ldsc4(rw, mloff);
    if (cg < 10) {
      v4u op[3];
      unsigned wp[3][8];
      if (__any(lane < 8 && bad4(ml, tag) != 0u)) {
#pragma unroll 1
        for (unsigned sp = 0; sp < SPIN_CAP; ++sp) {
          MEMCLOB();
          ml = ldsc4(rw, mloff);
          if (!__any(lane < 8 && bad4(ml, tag) != 0u)) break;
          __builtin_amdgcn_s_sleep(2);
        }
      }
      float wv;
      {
        float M = f32(ml.x); M = fmaxf(M, dpp<XOR1>(M)); M = fmaxf(M, dpp<XOR2>(M)); M = fmaxf(M, dpp<XOR7>(M));
        const float e = c < 6 ? __builtin_amdgcn_exp2f((f32(ml.x) - M) * LOG2E) : 0.f;
        wv = e / sum8(e * f32(ml.z));
        asm volatile("" : "+v"(wv));
      }
#pragma unroll
      for (int i = 0; i < 3; ++i) op[i] = ldsc4(rw, opoff, i * 2048);
      SB();
      float w3[3];
#pragma unroll
      for (int i = 0; i < 3; ++i) w3[i] = f32((unsigned)__builtin_amdgcn_ds_bpermute((3 * hf + i) * 4, (int)u32(wv)));
#pragma unroll
      for (int i = 0; i < 3; ++i) pairperm(wr[2 * i], wr[2 * i + 1], wp[i]);
      const v4u t4 = tag4(tag);
      unsigned tg0 = (lane & 1) ? t4.z : t4.x, tg1 = (lane & 1) ? t4.w : t4.y;
      asm volatile("" : "+v"(w3[0]), "+v"(w3[1]), "+v"(w3[2]), "+v"(tg0), "+v"(tg1));
      if (__any((bad4(op[0], tag) | bad4(op[1], tag) | bad4(op[2], tag)) != 0u)) {
#pragma unroll 1
        for (unsigned sp = 0; sp < SPIN_CAP; ++sp) {
          MEMCLOB();
#pragma unroll
          for (int i = 0; i < 3; ++i) op[i] = ldsc4(rw, opoff, i * 2048);
          if (!__any((bad4(op[0], tag) | bad4(op[1], tag) | bad4(op[2], tag)) != 0u)) break;
          __builtin_amdgcn_s_sleep(1);
        }
      }
      unsigned ov[3];
      {
        float o0 = fmaf(w3[2], f32(op[2].x), fmaf(w3[1], f32(op[1].x), w3[0] * f32(op[0].x))), o1 = fmaf(w3[2], f32(op[2].z), fmaf(w3[1], f32(op[1].z), w3[0] * f32(op[0].z)));
        o0 = fold32(o0, o0); o1 = fold32(o1, o1);
        const int opk = (int)pack2(o0, o1);
#pragma unroll
        for (int i = 0; i < 3; ++i) ov[i] = (unsigned)__builtin_amdgcn_ds_bpermute((8 * i + r) * 4, opk);
      }
      float a[8];
#pragma unroll
      for (int k = 0; k < 8; ++k) a[k] = dot2(wp[2][k], ov[2], dot2(wp[1][k], ov[1], dot2(wp[0][k], ov[0], 0.f)));
      float y[4], z[2];
#pragma unroll
      for (int i = 0; i < 4; ++i) y[i] = fold32(a[i], a[i + 4]);
#pragma unroll
      for (int i = 0; i < 2; ++i) { z[i] = fold16(y[i], y[i + 2]); z[i] += dpp<XOR8>(z[i]); }
      *(LAS float*)(L + a_ww) = (lane & 8) ? z[1] : z[0];
      lds_barrier();
      if (w < 4) {
        const LAS v2f* red = (const LAS v2f*)(L + LDS_VEC) + (lane & 31);
        v2f t[8];
#pragma unroll
        for (int i = 0; i < 8; ++i) t[i] = red[i * 32];
        const v2f o = ((t[0] + t[1]) + (t[2] + t[3])) + ((t[4] + t[5]) + (t[6] + t[7]));
        stsc2(v2u{pk24(o.x, tg0), pk24(o.y, tg1)}, rw, a_wp);
      }
    }
  } else {
    const int b = blockIdx.x, xg = px, v = pk;
    const int t320 = tid < 320 ? tid : 319;
    unsigned tag, cpair;
#define LDC() do { MEMCLOB(); cpair = ((const GAS unsigned*)cvec)[t320]; tag = ldsc1(rw, WS_EP); SB(); } while (0)
    unsigned xpair, xall[5];
    float ln0, ln1;
    v4u u0[10], u1[10], sg[4];
    unsigned p0[5][8], p1[5][8];
    const int rp = lane & 31, hf = lane >> 5;
#define FASTG(p) ((p) + ((p) + 4) / 7)
#define LDX() do { xpair = ((const GAS unsigned*)x)[t320]; _Pragma("unroll") for (int i = 0; i < 5; ++i) xall[i] = ((const GAS unsigned*)x)[lane + 64 * i]; SB(); } while (0)
#define SILU() do { asm volatile("" ::"v"(pf[0]), "v"(pf[1]), "v"(pf[2]), "v"(pf[3])); tag += 1u; const float c0 = lo16(cpair), c1 = hi16(cpair); \
      ((LAS unsigned*)(L + LDS_VEC))[t320] = pack2(c0 / (1.f + expf(-c0)), c1 / (1.f + expf(-c1))); SB(); } while (0)
#define LNX() do { float sm = 0.f; \
      _Pragma("unroll") for (int i = 0; i < 5; ++i) sm += lo16(xall[i]) + hi16(xall[i]); \
      const float mean = wave_sum(sm) * (1.f / D); float sq = 0.f; \
      _Pragma("unroll") for (int i = 0; i < 5; ++i) { const float a = lo16(xall[i]) - mean, bb = hi16(xall[i]) - mean; sq += a * a + bb * bb; } \
      const float var = wave_sum(sq) * (1.f / D); \
      const float mb = rbf(mean), sd = rbf(sqrtf(rbf(rbf(var) + 1e-5f))); \
      ln0 = rbf(rbf(lo16(xpair) - mb) / sd); ln1 = rbf(rbf(hi16(xpair) - mb) / sd); \
      asm volatile("" : "+v"(ln0), "+v"(ln1)); SB(); } while (0)
#define LDSAG() do { _Pragma("unroll") for (int i = 0; i < 4; ++i) sg[i] = ldsc4(rw, moff(tid < 320, WS_SAG + tid * 16), (i & 1) * 10240 + (i >> 1) * 5120); } while (0)
#define MAKEH() do { if (w < 5) { \
      if (__any((bad4(sg[0], tag) | bad4(sg[1], tag) | bad4(sg[2], tag) | bad4(sg[3], tag)) != 0u)) { \
        _Pragma("unroll 1") for (unsigned sp = 0; sp < SPIN_CAP; ++sp) { \
          MEMCLOB(); LDSAG(); \
          if (!__any((bad4(sg[0], tag) | bad4(sg[1], tag) | bad4(sg[2], tag) | bad4(sg[3], tag)) != 0u)) break; \
          __builtin_amdgcn_s_sleep(1); } } \
      const float sa0 = rbf(f32(sg[0].x) + f32(sg[1].x)), sa1 = rbf(f32(sg[0].z) + f32(sg[1].z)); \
      const float ca0 = rbf(f32(sg[2].x) + f32(sg[3].x)), ca1 = rbf(f32(sg[2].z) + f32(sg[3].z)); \
      ((LAS unsigned*)(L + LDS_VEC2))[tid] = pack2(rbf(ln0 * rbf(1.f + ca0)) + sa0, rbf(ln1 * rbf(1.f + ca1)) + sa1); } \
      lds_barrier(); } while (0)
    if (b < 40) {
      const int wi = 5 * xg + v, ga_ = wi < 20 ? wi : wi - 20, ha_ = wi < 20 ? 0 : 1, uu = b - 8;
      const bool two = v == 0 || (uu >= 12 && uu < 18);
      const int g0 = v == 0 ? FASTG(20 + xg) : (uu < 12 ? 8 * uu + 3 : FASTG(uu - 12));
      const int g1 = v == 0 ? FASTG(28 + xg) : FASTG(24 + uu);
      v4u ua[6];
      LDC();
      LDX();
      {
        const rsrc_t ra = mkrs(wmod + (size_t)(320 * ha_ + 40 * w) * (6 * D), 40 * 6 * D * 2);
#pragma unroll
        for (int i = 0; i < 6; ++i) ua[i] = ldnt(ra, 2 * r * (6 * D * 2) + ga_ * 128 + c * 16, (16 * (i >> 1) + (i & 1)) * (6 * D * 2));
        SB();
      }
      SILU();
      lds_barrier();
      unsigned hv[3];
      {
        const LAS unsigned* sc = (const LAS unsigned*)(L + LDS_VEC) + 160 * ha_ + 20 * w + r;
        hv[0] = sc[0]; hv[1] = sc[8]; hv[2] = sc[16]; hv[2] = r < 4 ? hv[2] : 0u;
      }
      unit_load<0, 5, true>(u0, wmod, wq, false, g0, w, lane);
      {
        unsigned pa[3][8];
#pragma unroll
        for (int i = 0; i < 3; ++i) pairperm(ua[2 * i], ua[2 * i + 1], pa[i]);
        float a[8];
#pragma unroll
        for (int k = 0; k < 8; ++k) a[k] = dot2(pa[2][k], hv[2], dot2(pa[1][k], hv[1], dot2(pa[0][k], hv[0], 0.f)));
        float y[4], z[2];
#pragma unroll
        for (int i = 0; i < 4; ++i) y[i] = fold32(a[i], a[i + 4]);
#pragma unroll
        for (int i = 0; i < 2; ++i) z[i] = fold16(y[i], y[i + 2]);
        const float t0 = z[0] + dpp<XOR8>(z[0]), t1 = z[1] + dpp<XOR8>(z[1]);
        ((LAS float*)(L + LDS_RED))[w * 64 + c * 8 + r] = (lane & 8) ? t1 : t0;
        lds_barrier();
        if (w == 0) {
          const LAS v2f* red = (const LAS v2f*)(L + LDS_RED) + (lane & 31);
          v2f sm = red[0];
#pragma unroll
          for (int i = 1; i < 8; ++i) sm += red[i * 32];
          stsc4(v4u{u32(sm.x), tag, u32(sm.y), tag}, rw, moff(lane < 32, WS_SAG + ha_ * 10240 + ga_ * 512 + lane * 16));
        }
      }
      SB();
      unit_load<5, 10, true>(u0, wmod, wq, false, g0, w, lane);
      LNX();
      if (two) unit_load<0, 10, true>(u1, wmod, wq, false, g1, w, lane);
      waitvm<2>();
      LDSAG();
      SB();
      unit_prep(u0, p0);
      if (two) unit_prep(u1, p1);
      SB();
      MAKEH();
      unit_partial(p0, L, LDS_VEC2, LDS_RED + 2048, w, lane);
      if (two) unit_partial(p1, L, LDS_VEC2, LDS_RED + 4096, w, lane);
      lds_barrier();
      if (w == 0 || (w == 1 && two)) unit_publish(L, LDS_RED + 2048 + w * 2048, lane, WS_QG + (w ? g1 : g0) * 256, tag, rw);
      SB();
      unit_load<0, 10, true>(u0, wmod, wq, true, 20 + b, w, lane);
      unit_prep(u0, p0);
      unit_partial(p0, L, LDS_VEC, LDS_RED, w, lane);
      lds_barrier();
      if (w == 0) unit_publish(L, LDS_RED, lane, WS_MODG + (20 + b) * 256, tag, rw);
    } else {
      const int f = 3 * px + pk - 5, fi = f - ((f + 4) >> 3);
      const int g0 = FASTG(42 + fi), g1 = FASTG(63 + fi);
      LDC();
      LDX();
      if ((f & 7) != 3) {
        unit_load<0, 6, false>(u0, wmod, wq, false, g0, w, lane);
        asm volatile("" : "+v"(tag), "+v"(xpair) : "v"(pf[0]), "v"(pf[1]), "v"(pf[2]), "v"(pf[3]), "v"(cpair));
        tag += 1u;
        unit_load<6, 10, true>(u0, wmod, wq, false, g0, w, lane);
        LNX();
        unit_load<0, 8, true>(u1, wmod, wq, false, g1, w, lane);
        unit_prep(u0, p0);
        unit_load<8, 10, true>(u1, wmod, wq, false, g1, w, lane);
        waitvm<2>();
        LDSAG();
        SB();
        unit_prep(u1, p1);
        SB();
        MAKEH();
        unit_partial(p0, L, LDS_VEC2, LDS_RED + 2048, w, lane);
        unit_partial(p1, L, LDS_VEC2, LDS_RED + 4096, w, lane);
        lds_barrier();
        if (w < 2) unit_publish(L, LDS_RED + 2048 + w * 2048, lane, WS_QG + (w ? g1 : g0) * 256, tag, rw);
        SB();
      } else {
        asm volatile("; no front work" : "+v"(tag), "+v"(xpair) : "v"(pf[0]), "v"(pf[1]), "v"(pf[2]), "v"(pf[3]), "v"(cpair));
        tag += 1u;
      }
      SB();
      const rsrc_t rd = mkrs(wd + (size_t)(f * 64 + 8 * w) * D, 8 * D * 2);
      const rsrc_t rg = mkrs(wg + f * 64, D * FF * 2 - f * 128), ru = mkrs(wu + f * 64, D * FF * 2 - f * 128);
      const int gvo = (40 * w + r) * (FF * 2) + c * 16;
      const int g4 = lane >> 4, q4 = (lane >> 2) & 3, p4 = lane & 3;
      v4u gu[20], dw[5][2];
#define LDD(nn, lo, hi) do { _Pragma("unroll") for (int i = lo; i < hi; ++i) { waitvm<CP>(); nn[i] = ldnt(rd, lane * 16, i * 1024); SB(); } } while (0)
#define LDGU(nn, p, lo, hi) do { _Pragma("unroll") for (int i = lo; i < hi; ++i) { waitvm<CP>(); nn[i] = ldnt(i < 5 ? rg : ru, gvo, (320 * (p) + 8 * (i % 5)) * FF * 2); SB(); } } while (0)
#define GUROUND(p, nn) do { \
        _Pragma("unroll") for (int i = 0; i < 5; ++i) { \
          const int row = 40 * w + 8 * i + r, a = row * 128 + ((c ^ ((row >> 1) & 7)) << 4); \
          *(LAS v4u*)(L + LDS_BIG + a) = nn[i]; *(LAS v4u*)(L + LDS_BIG + 40960 + a) = nn[5 + i]; } \
        lds_barrier(); \
        { const int row0 = 8 * g4 + q4, row1 = row0 + 4; \
          const LAS unsigned char* q0 = L + LDS_BIG + (p4 >> 1) * 40960 + row0 * 128 + ((w ^ ((row0 >> 1) & 7)) << 4) + (p4 & 1) * 8; \
          const LAS unsigned char* q1 = L + LDS_BIG + (p4 >> 1) * 40960 + row1 * 128 + ((w ^ ((row1 >> 1) & 7)) << 4) + (p4 & 1) * 8; \
          _Pragma("unroll") for (int ks = 0; ks < 10; ++ks) gu[(p) * 10 + ks] = trfrag(q0 + ks * 4096, q1 + ks * 4096); } \
        SB(); } while (0)
      LDGU(u0, 0, 0, 10);
      LDGU(u1, 1, 0, 6);
      GUROUND(0, u0);
      LDGU(u1, 1, 6, 10);
      LDD(u0, 0, 6);
      lds_barrier();
      GUROUND(1, u1);
      LDD(u0, 6, 10);
      lds_barrier();
#pragma unroll
      for (int i = 0; i < 10; ++i) *(LAS v4u*)(L + LDS_BIG + (10 * w + i) * 1024 + lane * 16) = u0[i];
      lds_barrier();
      {
        const LAS unsigned char* q0 = L + LDS_BIG + (8 * g4 + q4) * (D * 2) + w * 160 + p4 * 8;
#pragma unroll
        for (int T = 0; T < 5; ++T)
#pragma unroll
          for (int ks = 0; ks < 2; ++ks) dw[T][ks] = trfrag(q0 + ks * 32 * D * 2 + T * 32, q0 + (ks * 32 + 4) * D * 2 + T * 32);
        SB();
      }
      const bool red_on = (w == 5 || w == 6) && f < 20;
      const int opr = 16 * f + 8 * (w - 5) + c;
      int dpo0 = WS_DP + (f * D + 80 * w + lane) * 8, dpo1 = moff(lane < 16, dpo0 + 512), dpr = WS_DP + r * (D * 8) + opr * 16;
      asm volatile("" : "+v"(dpo0), "+v"(dpo1), "+v"(dpr));
      LAS v2f* stat = (LAS v2f*)(L + LDS_QS + 1024);
      float x20 = 0.f, x21 = 0.f;
      v2u ma, mb2, mc;
      const int pi = (tid & ~63) + 2 * rp + hf;
      xpair = (unsigned)__builtin_amdgcn_ds_bpermute((2 * rp + hf) * 4, (int)xpair);
      if (w >= 5) {
        stat[w] = v2f{0.f, 0.f};
        if (red_on) {
#pragma unroll 1
          for (unsigned sp = 0; sp < SPIN_CAP; ++sp) {
            MEMCLOB();
            ma = ldsc2(rw, WS_MODG + (1600 + opr) * 8);
            if (!__any(ma.y != tag)) break;
            __builtin_amdgcn_s_sleep(4);
          }
        }
      } else {
        v4u pv[8];
        unsigned miss = 0x1ffu;
        const int wpoff = WS_WP + px * WP_REP + (hf * D + 128 * w + 4 * rp) * 4;
        const v4u t4 = tag4(tag);
#pragma unroll 1
        for (unsigned sp = 0; sp < SPIN_CAP && miss; ++sp) {
          MEMCLOB();
#pragma unroll
          for (int g = 0; g < 8; ++g) if (__builtin_expect(miss & (1u << g), 0)) pv[g] = ldsc4(rw, wpoff, (2 * g) * D * 4);
          if (__builtin_expect(miss & 256u, 0)) { ma = ldsc2(rw, WS_MODG + (640 + pi) * 8); mb2 = ldsc2(rw, WS_MODG + (960 + pi) * 8); mc = ldsc2(rw, WS_MODG + (1280 + pi) * 8); }
#pragma unroll
          for (int g = 0; g < 8; ++g) if (__builtin_expect(miss & (1u << g), 0)) {
            const v4u e = pv[g] ^ t4;
            if (!__any(((e.x | e.y | e.z | e.w) & 0xffu) != 0u)) {
              miss &= ~(1u << g);
              pv[g].x = u32(fold32(f32(e.x), f32(e.z))); pv[g].y = u32(fold32(f32(e.y), f32(e.w)));
            }
          }
          if (__builtin_expect(miss & 256u, 0)) {
            if (!__any(((ma.y ^ tag) | (mb2.y ^ tag) | (mc.y ^ tag)) != 0u)) { miss &= 255u; mc.x = pack2(1.f + lo16(mc.x), 1.f + hi16(mc.x)); }
          }
        }
        const v2f at = {((f32(pv[0].x) + f32(pv[1].x)) + (f32(pv[2].x) + f32(pv[3].x))) + ((f32(pv[4].x) + f32(pv[5].x)) + (f32(pv[6].x) + f32(pv[7].x))),
                        ((f32(pv[0].y) + f32(pv[1].y)) + (f32(pv[2].y) + f32(pv[3].y))) + ((f32(pv[4].y) + f32(pv[5].y)) + (f32(pv[6].y) + f32(pv[7].y)))};
        x20 = rbf(lo16(xpair) + rbf(lo16(ma.x) * rbf(at.x))); x21 = rbf(hi16(xpair) + rbf(hi16(ma.x) * rbf(at.y)));
        *(LAS v2f*)(L + LDS_VEC2 + pi * 8) = v2f{x20, x21};
        const float piv = __builtin_bit_cast(float, __builtin_amdgcn_readfirstlane(__builtin_bit_cast(int, x20)));
        const float d0 = x20 - piv, d1 = x21 - piv;
        const float s1 = wave_sum(d0 + d1), s2 = wave_sum(d0 * d0 + d1 * d1);
        stat[w] = v2f{piv + s1 * (1.f / 128), s2 - s1 * s1 * (1.f / 128)};
      }
      lds_barrier();
      if (w < 5) {
        const v2f st = stat[c];
        const float mean = sum8(st.x) * 0.2f, dm = st.x - mean;
        const float m2 = sum8(c < 5 ? fmaf(128.f * dm, dm, st.y) : 0.f);
        const float mb = rbf(mean), sd = rbf(__builtin_amdgcn_sqrtf(rbf(rbf(fmaxf(m2, 0.f) * (1.f / D)) + 1e-5f)));
        const float rs = __builtin_amdgcn_rcpf(sd);
        const float l0 = rbf(rbf(x20 - mb) * rs), l1 = rbf(rbf(x21 - mb) * rs);
        ((LAS unsigned*)(L + LDS_HB))[pi] = pack2(rbf(l0 * lo16(mc.x)) + lo16(mb2.x), rbf(l1 * hi16(mc.x)) + hi16(mb2.x));
      }
      lds_barrier();
      {
        const LAS v4u* hb = (const LAS v4u*)(L + LDS_HB) + g4;
        v4u ha[20];
#pragma unroll
        for (int ks = 0; ks < 20; ++ks) ha[ks] = hb[4 * ks];
        SB();
        v4f acc = {0.f, 0.f, 0.f, 0.f};
#pragma unroll
        for (int ks = 0; ks < 20; ++ks) acc = mfma16(ha[ks], gu[ks], acc);
        const float vv = rbf(acc.x);
        ((LAS __bf16*)(L + LDS_QS))[8 * w + c] = (__bf16)(vv * dpp<XOR8>(vv));
      }
      lds_barrier();
      {
        const LAS v4u* hp2 = (const LAS v4u*)(L + LDS_QS) + g4;
        const v4u h0 = hp2[0], h1 = hp2[4];
        v4f acc[5];
#pragma unroll
        for (int T = 0; T < 5; ++T) acc[T] = mfma16(h0, dw[T][0], v4f{0.f, 0.f, 0.f, 0.f});
#pragma unroll
        for (int T = 0; T < 5; ++T) acc[T] = mfma16(h1, dw[T][1], acc[T]);
        const float z = (lane & 32) ? ((lane & 16) ? acc[3].x : acc[2].x) : ((lane & 16) ? acc[1].x : acc[0].x);
        stsc2(v2u{u32(z), tag}, rw, dpo0);
        stsc2(v2u{u32(acc[4].x), tag}, rw, dpo1);
      }
      if (red_on) {
        const v2f x2v = ((const LAS v2f*)(L + LDS_VEC2))[opr];
        v4u dv[3];
#pragma unroll 1
        for (unsigned sp = 0; sp < SPIN_CAP; ++sp) {
          MEMCLOB();
#pragma unroll
          for (int i = 0; i < 3; ++i) dv[i] = __builtin_amdgcn_raw_buffer_load_b128(rw, dpr, i * 8 * D * 8, AUX_SC1);
          if (!__any((bad4(dv[0], tag) | bad4(dv[1], tag) | bad4(dv[2], tag)) != 0u)) break;
        }
        float z0 = (f32(dv[0].x) + f32(dv[1].x)) + f32(dv[2].x), z1 = (f32(dv[0].z) + f32(dv[1].z)) + f32(dv[2].z);
        z0 += dpp<XOR8>(z0); z1 += dpp<XOR8>(z1);
        z0 = fold16(z0, z0); z1 = fold16(z1, z1); z0 = fold32(z0, z0); z1 = fold32(z1, z1);
        const unsigned o = pack2(x2v.x + rbf(lo16(ma.x) * rbf(z0)), x2v.y + rbf(hi16(ma.x) * rbf(z1)));
        __builtin_amdgcn_raw_buffer_store_b32(o, mkrs(out, D * 2), moff(lane < 8, opr * 4), 0, 0);
        if (f == 0 && w == 5) stsc1(tag, rw, moff(lane == 0, WS_EP));
      }
    }
  }
}

extern "C" void kernel_launch(void* const* d_in, const int* in_sizes, int n_in, void* d_out, int out_size, void* d_ws, size_t ws_size,
                              hipStream_t stream) {
  hipLaunchKernelGGL(mega, dim3(256), dim3(512), 0, stream, (const u16*)d_in[0], (const u16*)d_in[1], (const u16*)d_in[2], (const u16*)d_in[3],
                     (const u16*)d_in[4], (const u16*)d_in[6], (const u16*)d_in[7], (const u16*)d_in[8], (const u16*)d_in[9], (const u16*)d_in[10],
                     (u16*)d_out, (unsigned char*)d_ws);
}
```

```cpp
#include <hip/hip_runtime.h>
#include <hip/hip_bf16.h>
#include <cstdint>

typedef unsigned short u16;
typedef unsigned v4u __attribute__((ext_vector_type(4)));
typedef unsigned v2u __attribute__((ext_vector_type(2)));
typedef float v4f __attribute__((ext_vector_type(4)));
typedef float v2f __attribute__((ext_vector_type(2)));
typedef __bf16 bf16x2 __attribute__((ext_vector_type(2)));
typedef short v4s __attribute__((ext_vector_type(4)));
typedef __bf16 bf16x8 __attribute__((ext_vector_type(8)));
typedef __amdgpu_buffer_rsrc_t rsrc_t;
#define LAS __attribute__((address_space(3)))
#define GAS __attribute__((address_space(1)))
#define DEV __device__ __forceinline__

#define PFKB 22
constexpr int CP = 5;
constexpr int SP = 2, SP2 = 5, BW = 3;
constexpr int TAU = 76, TA0 = -30;

constexpr int D = 640, SK = 4096, DH = 192, FF = 1536;
constexpr int AUX_NT = 2, AUX_SC1 = 16;
constexpr int WS_EP = 0, WS_MODG = 4096, WS_QG = 32768, WS_MLG = 65536, WS_OP = 131072, WS_WP = 1310720, WP_REP = 40960, WS_SAG = 98304, WS_DP = 786432, WS_BYTES = 2 << 20;
constexpr unsigned SPIN_CAP = 1u << 17;
constexpr float LOG2E = 1.4426950408889634f;
constexpr float QSCALE = 0.07216878364870322f;

constexpr int LDS_RED = 0;
constexpr int LDS_VEC = 6144;
constexpr int LDS_VEC2 = 8704;
constexpr int LDS_HB = 11264;
constexpr int LDS_QS = 12800;
constexpr int LDS_BIG = 16896;
constexpr int LDS_BYTES = 16896 + 64 * 1312;

DEV float lo16(unsigned w) { return __uint_as_float(w << 16); }
DEV float hi16(unsigned w) { return __uint_as_float(w & 0xffff0000u); }
DEV float f32(unsigned w) { return __uint_as_float(w); }
DEV unsigned u32(float f) { return __float_as_uint(f); }
DEV float rbf(float f) { return (float)(__bf16)f; }
DEV unsigned pack2(float a, float b) { bf16x2 v = {(__bf16)a, (__bf16)b}; return __builtin_bit_cast(unsigned, v); }
DEV float dot2(unsigned a, unsigned b, float acc) { return __builtin_amdgcn_fdot2_f32_bf16(__builtin_bit_cast(bf16x2, a), __builtin_bit_cast(bf16x2, b), acc, false); }
template <int CTRL> DEV float dpp(float x) { return __builtin_bit_cast(float, __builtin_amdgcn_mov_dpp(__builtin_bit_cast(int, x), CTRL, 0xf, 0xf, true)); }
constexpr int XOR1 = 0xB1, XOR2 = 0x4E, XOR7 = 0x141, XOR8 = 0x128;
DEV float fold32(float a, float b) { auto s = __builtin_amdgcn_permlane32_swap(u32(a), u32(b), false, false); return f32(s[0]) + f32(s[1]); }
DEV float fold16(float a, float b) { auto s = __builtin_amdgcn_permlane16_swap(u32(a), u32(b), false, false); return f32(s[0]) + f32(s[1]); }
DEV float max32(float a) { auto s = __builtin_amdgcn_permlane32_swap(u32(a), u32(a), false, false); return fmaxf(f32(s[0]), f32(s[1])); }
DEV float max16(float a) { auto s = __builtin_amdgcn_permlane16_swap(u32(a), u32(a), false, false); return fmaxf(f32(s[0]), f32(s[1])); }
DEV float wave_sum(float x) { x += dpp<XOR1>(x); x += dpp<XOR2>(x); x += dpp<XOR7>(x); x += dpp<XOR8>(x); x = fold16(x, x); return fold32(x, x); }
DEV rsrc_t mkrs(const void* p, int bytes) {
  const unsigned long long a = (unsigned long long)p;
  const unsigned long long u = ((unsigned long long)(unsigned)__builtin_amdgcn_readfirstlane((int)(a >> 32)) << 32) | (unsigned)__builtin_amdgcn_readfirstlane((int)a);
  return __builtin_amdgcn_make_buffer_rsrc((void*)u, 0, __builtin_amdgcn_readfirstlane(bytes), 0x00020000);
}
DEV v4u ldnt(rsrc_t rs, int voff, int soff) { return __builtin_amdgcn_raw_buffer_load_b128(rs, voff, soff, AUX_NT); }
#define MEMCLOB() asm volatile("" ::: "memory")
DEV v4u ldsc4(rsrc_t rs, int voff, int soff = 0) { return __builtin_amdgcn_raw_buffer_load_b128(rs, voff, soff, AUX_SC1); }
DEV v2u ldsc2(rsrc_t rs, int voff, int soff = 0) { return __builtin_amdgcn_raw_buffer_load_b64(rs, voff, soff, AUX_SC1); }
DEV unsigned ldsc1(rsrc_t rs, int voff) { return __builtin_amdgcn_raw_buffer_load_b32(rs, voff, 0, AUX_SC1); }
DEV void stsc4(v4u v, rsrc_t rs, int voff) { __builtin_amdgcn_raw_buffer_store_b128(v, rs, voff, 0, AUX_SC1); }
DEV void stsc2(v2u v, rsrc_t rs, int voff) { __builtin_amdgcn_raw_buffer_store_b64(v, rs, voff, 0, AUX_SC1); }
DEV void stsc1(unsigned v, rsrc_t rs, int voff) { __builtin_amdgcn_raw_buffer_store_b32(v, rs, voff, 0, AUX_SC1); }
DEV int moff(bool on, int off) { return on ? off : 0x40000000; }
DEV v4u tag4(unsigned tag) { return v4u{tag & 0xffu, (tag >> 8) & 0xffu, (tag + (tag >> 16)) & 0xffu, tag >> 24}; }
DEV unsigned pk24(float f, unsigned t8) { const unsigned u = u32(f); return ((u + 0x7fu + ((u >> 8) & 1u)) & 0xffffff00u) | t8; }
DEV unsigned bad4(v4u v, unsigned tag) { return (v.y ^ tag) | (v.w ^ tag); }
#define PIN10(a) asm volatile("" : "+v"((a)[0]), "+v"((a)[1]), "+v"((a)[2]), "+v"((a)[3]), "+v"((a)[4]), "+v"((a)[5]), "+v"((a)[6]), "+v"((a)[7]), "+v"((a)[8]), "+v"((a)[9]))
#define PIN24(a) asm volatile("" : "+v"(a[0]), "+v"(a[1]), "+v"(a[2]), "+v"(a[3]), "+v"(a[4]), "+v"(a[5]), "+v"(a[6]), "+v"(a[7]), "+v"(a[8]), "+v"(a[9]), "+v"(a[10]), "+v"(a[11]), \
  "+v"(a[12]), "+v"(a[13]), "+v"(a[14]), "+v"(a[15]), "+v"(a[16]), "+v"(a[17]), "+v"(a[18]), "+v"(a[19]), "+v"(a[20]), "+v"(a[21]), "+v"(a[22]), "+v"(a[23]))
template <int N> DEV void waitvm() { asm volatile("s_waitcnt vmcnt(%0)" ::"i"(N) : "memory"); }
#define SB() __builtin_amdgcn_sched_barrier(0)
DEV void lds_barrier() { asm volatile("s_waitcnt lgkmcnt(0)" ::: "memory"); __builtin_amdgcn_s_barrier(); asm volatile("" ::: "memory"); }
DEV void lds_fence() { asm volatile("s_waitcnt lgkmcnt(0)" ::: "memory"); }

DEV float sum8(float x) { x += dpp<XOR1>(x); x += dpp<XOR2>(x); x += dpp<XOR7>(x); return x; }
DEV v4u trfrag(const LAS unsigned char* p0, const LAS unsigned char* p1) {
  const v2u a = __builtin_bit_cast(v2u, __builtin_amdgcn_ds_read_tr16_b64_v4i16((LAS v4s*)p0)), b = __builtin_bit_cast(v2u, __builtin_amdgcn_ds_read_tr16_b64_v4i16((LAS v4s*)p1));
  return v4u{a.x, a.y, b.x, b.y};
}
DEV v4f mfma16(v4u a, v4u b, v4f c) { return __builtin_amdgcn_mfma_f32_16x16x32_bf16(__builtin_bit_cast(bf16x8, a), __builtin_bit_cast(bf16x8, b), c, 0, 0, 0); }
DEV void pairperm(v4u e, v4u o, unsigned (&p)[8]) {
#pragma unroll
  for (int k = 0; k < 4; ++k) { p[2 * k] = __builtin_amdgcn_perm(o[k], e[k], 0x05040100u); p[2 * k + 1] = __builtin_amdgcn_perm(o[k], e[k], 0x07060302u); }
  asm volatile("" : "+v"(p[0]), "+v"(p[1]), "+v"(p[2]), "+v"(p[3]), "+v"(p[4]), "+v"(p[5]), "+v"(p[6]), "+v"(p[7]));
}
DEV void fma8(float (&acc)[24], int base, float p, v4u v) {
#pragma unroll
  for (int k = 0; k < 4; ++k) { acc[base + 2 * k] = fmaf(p, lo16(v[k]), acc[base + 2 * k]); acc[base + 2 * k + 1] = fmaf(p, hi16(v[k]), acc[base + 2 * k + 1]); }
}
DEV void unit_prep(const v4u (&raw)[10], unsigned (&p)[5][8]) {
#pragma unroll
  for (int i = 0; i < 5; ++i) pairperm(raw[2 * i], raw[2 * i + 1], p[i]);
}
DEV void unit_partial(const unsigned (&p)[5][8], LAS unsigned char* L, int vecoff, int redoff, int w, int lane) {
  const LAS unsigned* sc = (const LAS unsigned*)(L + vecoff) + 40 * w + (lane >> 3);
  float a[8];
#pragma unroll
  for (int k = 0; k < 8; ++k) a[k] = 0.f;
#pragma unroll
  for (int i = 0; i < 5; ++i) { const unsigned hv = sc[8 * i];
#pragma unroll
    for (int k = 0; k < 8; ++k) a[k] = dot2(p[i][k], hv, a[k]); }
  float y[4], z[2];
#pragma unroll
  for (int i = 0; i < 4; ++i) y[i] = fold32(a[i], a[i + 4]);
#pragma unroll
  for (int i = 0; i < 2; ++i) z[i] = fold16(y[i], y[i + 2]);
  const float t0 = z[0] + dpp<XOR8>(z[0]), t1 = z[1] + dpp<XOR8>(z[1]);
  ((LAS float*)(L + redoff))[w * 64 + (lane & 7) * 8 + (lane >> 3)] = (lane & 8) ? t1 : t0;
}
DEV v4u ldp(rsrc_t rs, int voff, int soff) { waitvm<CP>(); const v4u v = ldnt(rs, voff, soff); SB(); return v; }
DEV void unit_publish(LAS unsigned char* L, int redoff, int lane, int outoff, unsigned tag, rsrc_t rw) {
  const LAS v2f* red = (const LAS v2f*)(L + redoff) + (lane & 31);
  v2f s = red[0];
#pragma unroll
  for (int i = 1; i < 8; ++i) s += red[i * 32];
  stsc2(v2u{pack2(s.x, s.y), tag}, rw, moff(lane < 32, outoff + lane * 8));
}
template <int LO, int HI, bool PACED> DEV void unit_load(v4u (&raw)[10], const u16* wmod, const u16* wq, bool is_mod, int g, int w, int lane) {
  const int stride = is_mod ? 6 * D * 2 : 32 * DH * 2;
  const rsrc_t rs = mkrs((const unsigned char*)(is_mod ? wmod : wq) + (size_t)80 * w * stride, 80 * stride);
  const int vo = (lane >> 3) * 2 * stride + g * 128 + (lane & 7) * 16;
#pragma unroll
  for (int i = LO; i < HI; ++i) raw[i] = (PACED ? ldp(rs, vo, (16 * (i >> 1) + (i & 1)) * stride) : ldnt(rs, vo, (16 * (i >> 1) + (i & 1)) * stride));
  SB();
}

__global__ void __launch_bounds__(512, 2)
mega(const u16* x, const u16* Kc, const u16* Vc, const u16* wq, const u16* wo,
     const u16* wg, const u16* wu, const u16* wd, const u16* cvec, const u16* wmod,
     u16* out, unsigned char* ws) {
  __shared__ __attribute__((aligned(16))) unsigned char smem[LDS_BYTES];
  LAS unsigned char* L = (LAS unsigned char*)smem;
  const int px = blockIdx.x & 7, pk = blockIdx.x >> 3;
  const int tid = threadIdx.x, lane = tid & 63, w = __builtin_amdgcn_readfirstlane(tid >> 6);
  const int r = lane >> 3, c = lane & 7;
  v4u pf[3];
  if (pk < 8) {
    const rsrc_t rc = mkrs((const void*)(__builtin_amdgcn_s_getpc() & ~127ull), PFKB * 1024);
#pragma unroll
    for (int i = 0; i < 3; ++i) pf[i] = __builtin_amdgcn_raw_buffer_load_b128(rc, tid * 16, i * 8192, 0);
    MEMCLOB();
  }
  const int ph_ = pk >= 20 ? 1 : 0, P = 2 * px + ph_, cg = pk - 8 - 12 * ph_, hq = cg >= 6 ? 1 : 0, s = cg - 6 * hq, h = 2 * P + hq;
  const int rem = s < 2 ? 6 : 5, t0cu = 85 * s + (s < 2 ? s : 2);
  const int nt = w < rem ? 11 : 10, TS = 8 * 3072;
  int nrec = (80 + rem - w) * 3072;
  unsigned kvoff = (unsigned)(h * 512 + t0cu + w) * 3072u;
  int rot = h % 10;
  asm volatile("" : "+s"(nrec), "+s"(kvoff), "+s"(rot));
#define TOFF(t) ((t) == 10 ? 10 * TS : ((t) + rot >= 10 ? (t) + rot - 10 : (t) + rot) * TS)
  asm volatile("" ::"s"(x), "s"(Kc), "s"(Vc), "s"(wq), "s"(wo), "s"(wg), "s"(wu), "s"(wd), "s"(cvec), "s"(wmod), "s"(out), "s"(ws));
  const rsrc_t rw = mkrs(ws, WS_BYTES);
  if (__builtin_expect(pk >= 8, 1)) {
    const rsrc_t rk = mkrs((const unsigned char*)Kc + kvoff, nrec), rv = mkrs((const unsigned char*)Vc + kvoff, nrec);
    const rsrc_t rwo = mkrs(wo + (size_t)(384 * P + 48 * w) * D + 64 * cg, cg < 10 ? 48 * D * 2 - 128 * cg : 0);
    const int voff = r * 384 + c * 16, l48 = lane < 48 ? lane : 47;
    v4u kr[11][3], vr[11][3];
    unsigned tag;
    const unsigned tst = (unsigned)__builtin_amdgcn_s_memrealtime() + TA0;
#define PACE(tt, lo) do { waitvm<(lo) + BW>(); if ((int)((unsigned)__builtin_amdgcn_s_memrealtime() - tst - (tt)) < 0) waitvm<lo>(); } while (0)
#define LDV(t) do { PACE(10 * TAU + 2 * TAU / 3 + (t) * TAU, SP); _Pragma("unroll") for (int j = 0; j < 3; ++j) vr[t][j] = ldnt(rv, voff + j * 128, TOFF(t)); SB(); } while (0)
#define LDVN(t) do { PACE(10 * TAU + 2 * TAU / 3 + (t) * TAU, SP2); _Pragma("unroll") for (int j = 0; j < 3; ++j) vr[t][j] = ldnt(rv, voff + j * 128, TOFF(t)); SB(); } while (0)
#define VPERM(u) do { _Pragma("unroll") for (int j = 0; j < 3; ++j) pairperm(vr[2 * (u)][j], vr[2 * (u) + 1][j], vp[u][j]); SB(); } while (0)
#define PV(u) do { _Pragma("unroll") for (int j = 0; j < 3; ++j) _Pragma("unroll") for (int k = 0; k < 8; ++k) acc[j * 8 + k] = dot2(vp[u][j][k], pp[u], acc[j * 8 + k]); PIN24(acc); SB(); } while (0)
#pragma unroll
    for (int t = 0; t < 11; ++t) {
      if (t >= 2) PACE(t * TAU, SP);
      if (t == 8) tag = ldsc1(rw, WS_EP);
#pragma unroll
      for (int j = 0; j < 3; ++j) { kr[t][j] = ldnt(rk, voff + j * 128, TOFF(t)); SB(); }
    }
    unsigned vp[5][3][8];
    v4u qg;
#pragma unroll
    for (int t = 0; t <= 6; ++t) {
      if (t == 5) { qg = ldsc4(rw, WS_QG + h * 768 + l48 * 16); SB(); }
      LDV(t);
      if (t == 0) { tag += 1u; asm volatile("" : "+v"(tag)); SB(); }
      if (t == 3) VPERM(0);
      if (t == 5) VPERM(1);
    }
    if (__any(bad4(qg, tag) != 0u)) {
#pragma unroll 1
      for (unsigned sp = 0; sp < SPIN_CAP; ++sp) {
        MEMCLOB();
        qg = ldsc4(rw, WS_QG + h * 768 + l48 * 16);
        if (!__any(bad4(qg, tag) != 0u)) break;
        __builtin_amdgcn_s_sleep(2);
      }
    }
    LAS unsigned* qs = (LAS unsigned*)(L + LDS_QS) + w * 96;
    *(LAS v2u*)(qs + 2 * l48) = v2u{qg.x, qg.z};
    lds_fence();
    v4u qp[3];
#pragma unroll
    for (int j = 0; j < 3; ++j) qp[j] = *(const LAS v4u*)(qs + j * 32 + c * 4);
    float sc[12];
#pragma unroll
    for (int t = 0; t < 11; ++t) {
      float d = 0.f;
#pragma unroll
      for (int j = 0; j < 3; ++j)
#pragma unroll
        for (int k = 0; k < 4; ++k) d = dot2(kr[t][j][k], qp[j][k], d);
      d += dpp<XOR1>(d); d += dpp<XOR2>(d); d += dpp<XOR7>(d);
      d = rbf(rbf(d) * QSCALE);
      sc[t] = (t < 10 || nt == 11) ? d : -INFINITY;
      asm volatile("" : "+v"(sc[t]));
      if (t == 4) { SB(); LDVN(7); }
      if (t == 10) { SB(); LDVN(8); }
    }
    float mw = sc[0];
#pragma unroll
    for (int t = 1; t < 11; ++t) mw = fmaxf(mw, sc[t]);
    mw = fmaxf(mw, dpp<XOR8>(mw)); mw = max16(mw); mw = max32(mw);
    float lw = 0.f;
#pragma unroll
    for (int t = 0; t < 11; ++t) { sc[t] = __builtin_amdgcn_exp2f((sc[t] - mw) * LOG2E); lw += sc[t]; }
    sc[11] = 0.f;
    lw += dpp<XOR8>(lw); lw = fold16(lw, lw); lw = fold32(lw, lw);
    unsigned pp[6];
#pragma unroll
    for (int u = 0; u < 6; ++u) pp[u] = pack2(sc[2 * u], sc[2 * u + 1]);
    ((LAS v2f*)(L + LDS_HB))[w] = v2f{mw, lw};
    float acc[24];
#pragma unroll
    for (int i = 0; i < 24; ++i) acc[i] = 0.f;
    v4u wr[6];
#define LDWO() do { _Pragma("unroll") for (int i = 0; i < 6; ++i) wr[i] = ldnt(rwo, r * (2 * D * 2) + c * 16, (16 * (i >> 1) + (i & 1)) * D * 2); SB(); } while (0)
    float fw;
#define MLPUB() do { \
      lds_barrier(); \
      const v2f ml = ((const LAS v2f*)(L + LDS_HB))[c]; \
      float M = ml.x; M = fmaxf(M, dpp<XOR1>(M)); M = fmaxf(M, dpp<XOR2>(M)); M = fmaxf(M, dpp<XOR7>(M)); \
      const float Lc = sum8(ml.y * __builtin_amdgcn_exp2f((ml.x - M) * LOG2E)); \
      fw = __builtin_amdgcn_exp2f((mw - M) * LOG2E); \
      asm volatile("" : "+v"(fw)); \
      if (w == 7) stsc4(v4u{u32(M), tag, u32(Lc), tag}, rw, moff(lane < 8, WS_MLG + (h * 8 + s) * 128 + lane * 16)); \
      SB(); } while (0)
    const int hh = 2 * P + (w >> 2), mloff = moff(lane < 8, WS_MLG + (hh * 8 + (c < 6 ? c : 5)) * 128);
    const int dp = 48 * (w & 1) + l48, hf = lane >> 5, p24 = (lane & 31) < 24 ? (lane & 31) : 23;
    int a_rw = LDS_RED + (w * 192 + c * 24 + r * 3) * 4, a_rr = LDS_RED + (((dp >> 2) & 7) * 12 + (dp >> 5) * 4 + (dp & 3)) * 8;
    int a_op = moff(lane < 48, WS_OP + (h * 8 + s) * 2048 + dp * 16), opoff = WS_OP + (hh * 8 + 3 * hf) * 2048 + (24 * (w & 3) + p24) * 16;
    int a_ww = LDS_VEC + (w * 64 + c * 8 + r) * 4, a_wp = WS_WP + (2 * w + hf) * WP_REP + (P * D + 64 * cg + 2 * (lane & 31)) * 4;
    asm volatile("" : "+v"(a_rw), "+v"(a_rr), "+v"(a_op), "+v"(opoff), "+v"(a_ww), "+v"(a_wp));
    MLPUB(); LDVN(9); PV(0); PV(1); VPERM(2); PV(2); LDVN(10); VPERM(3); PV(3);
    VPERM(4); PV(4);
    LDWO();
#pragma unroll
    for (int j = 0; j < 3; ++j) fma8(acc, j * 8, lo16(pp[5]), vr[10][j]);
    PIN24(acc); SB();
    {
      float y[12], z[6];
#pragma unroll
      for (int i = 0; i < 12; ++i) y[i] = fold32(acc[i], acc[i + 12]);
#pragma unroll
      for (int i = 0; i < 6; ++i) z[i] = fold16(y[i], y[i + 6]);
      LAS float* red = (LAS float*)(L + a_rw);
#pragma unroll
      for (int i = 0; i < 3; ++i) { const float t0 = z[i] + dpp<XOR8>(z[i]), t1 = z[i + 3] + dpp<XOR8>(z[i + 3]); red[i] = ((lane & 8) ? t1 : t0) * fw; }
    }
    SB();
    lds_barrier();
    if (w < 2) {
      const LAS v2f* red = (const LAS v2f*)(L + a_rr);
      v2f t[8];
#pragma unroll
      for (int i = 0; i < 8; ++i) t[i] = red[i * 96];
      const v2f o = ((t[0] + t[1]) + (t[2] + t[3])) + ((t[4] + t[5]) + (t[6] + t[7]));
      stsc4(v4u{u32(o.x), tag, u32(o.y), tag}, rw, a_op);
    }
    SB();
    v4u ml = ldsc4(rw, mloff);
    if (cg < 10) {
      v4u op[3];
      unsigned wp[3][8];
      if (__any(lane < 8 && bad4(ml, tag) != 0u)) {
#pragma unroll 1
        for (unsigned sp = 0; sp < SPIN_CAP; ++sp) {
          MEMCLOB();
          ml = ldsc4(rw, mloff);
          if (!__any(lane < 8 && bad4(ml, tag) != 0u)) break;
          __builtin_amdgcn_s_sleep(2);
        }
      }
      float wv;
      {
        float M = f32(ml.x); M = fmaxf(M, dpp<XOR1>(M)); M = fmaxf(M, dpp<XOR2>(M)); M = fmaxf(M, dpp<XOR7>(M));
        const float e = c < 6 ? __builtin_amdgcn_exp2f((f32(ml.x) - M) * LOG2E) : 0.f;
        wv = e / sum8(e * f32(ml.z));
        asm volatile("" : "+v"(wv));
      }
#pragma unroll
      for (int i = 0; i < 3; ++i) op[i] = ldsc4(rw, opoff, i * 2048);
      SB();
      float w3[3];
#pragma unroll
      for (int i = 0; i < 3; ++i) w3[i] = f32((unsigned)__builtin_amdgcn_ds_bpermute((3 * hf + i) * 4, (int)u32(wv)));
#pragma unroll
      for (int i = 0; i < 3; ++i) pairperm(wr[2 * i], wr[2 * i + 1], wp[i]);
      const v4u t4 = tag4(tag);
      unsigned tg0 = (lane & 1) ? t4.z : t4.x, tg1 = (lane & 1) ? t4.w : t4.y;
      asm volatile("" : "+v"(w3[0]), "+v"(w3[1]), "+v"(w3[2]), "+v"(tg0), "+v"(tg1));
      float a[8] = {0.f, 0.f, 0.f, 0.f, 0.f, 0.f, 0.f, 0.f};
      int a_bp = r * 4;
      asm volatile("" : "+v"(a[0]), "+v"(a[1]), "+v"(a[2]), "+v"(a[3]), "+v"(a[4]), "+v"(a[5]), "+v"(a[6]), "+v"(a[7]), "+v"(a_bp));
      if (__any((bad4(op[0], tag) | bad4(op[1], tag) | bad4(op[2], tag)) != 0u)) {
#pragma unroll 1
        for (unsigned sp = 0; sp < SPIN_CAP; ++sp) {
          MEMCLOB();
#pragma unroll
          for (int i = 0; i < 3; ++i) op[i] = ldsc4(rw, opoff, i * 2048);
          if (!__any((bad4(op[0], tag) | bad4(op[1], tag) | bad4(op[2], tag)) != 0u)) break;
          __builtin_amdgcn_s_sleep(1);
        }
      }
      unsigned ov[3];
      {
        float o0 = fmaf(w3[2], f32(op[2].x), fmaf(w3[1], f32(op[1].x), w3[0] * f32(op[0].x))), o1 = fmaf(w3[2], f32(op[2].z), fmaf(w3[1], f32(op[1].z), w3[0] * f32(op[0].z)));
        o0 = fold32(o0, o0); o1 = fold32(o1, o1);
        const int opk = (int)pack2(o0, o1);
#pragma unroll
        for (int i = 0; i < 3; ++i) ov[i] = (unsigned)__builtin_amdgcn_ds_bpermute(a_bp + 32 * i, opk);
      }
#pragma unroll
      for (int i = 0; i < 3; ++i)
#pragma unroll
        for (int k = 0; k < 8; ++k) a[k] = dot2(wp[i][k], ov[i], a[k]);
      float y[4], z[2];
#pragma unroll
      for (int i = 0; i < 4; ++i) y[i] = fold32(a[i], a[i + 4]);
#pragma unroll
      for (int i = 0; i < 2; ++i) { z[i] = fold16(y[i], y[i + 2]); z[i] += dpp<XOR8>(z[i]); }
      *(LAS float*)(L + a_ww) = (lane & 8) ? z[1] : z[0];
      lds_barrier();
      if (w < 4) {
        const LAS v2f* red = (const LAS v2f*)(L + LDS_VEC) + (lane & 31);
        v2f t[8];
#pragma unroll
        for (int i = 0; i < 8; ++i) t[i] = red[i * 32];
        const v2f o = ((t[0] + t[1]) + (t[2] + t[3])) + ((t[4] + t[5]) + (t[6] + t[7]));
        stsc2(v2u{pk24(o.x, tg0), pk24(o.y, tg1)}, rw, a_wp);
      }
    }
  } else {
    const int b = blockIdx.x, xg = px, v = pk;
    const int t320 = tid < 320 ? tid : 319;
    unsigned tag, cpair;
#define LDC() do { MEMCLOB(); cpair = ((const GAS unsigned*)cvec)[t320]; tag = ldsc1(rw, WS_EP); SB(); } while (0)
    unsigned xpair, xall[5];
    float ln0, ln1;
    v4u u0[10], u1[10], sg[4];
    unsigned p0[5][8], p1[5][8];
    const int rp = lane & 31, hf = lane >> 5;
#define FASTG(p) ((p) + ((p) + 4) / 7)
#define LDX() do { xpair = ((const GAS unsigned*)x)[t320]; _Pragma("unroll") for (int i = 0; i < 5; ++i) xall[i] = ((const GAS unsigned*)x)[lane + 64 * i]; SB(); } while (0)
#define SILU() do { asm volatile("" ::"v"(pf[0]), "v"(pf[1]), "v"(pf[2])); tag += 1u; const float c0 = lo16(cpair), c1 = hi16(cpair); \
      ((LAS unsigned*)(L + LDS_VEC))[t320] = pack2(c0 / (1.f + expf(-c0)), c1 / (1.f + expf(-c1))); SB(); } while (0)
#define LNX() do { float sm = 0.f; \
      _Pragma("unroll") for (int i = 0; i < 5; ++i) sm += lo16(xall[i]) + hi16(xall[i]); \
      const float mean = wave_sum(sm) * (1.f / D); float sq = 0.f; \
      _Pragma("unroll") for (int i = 0; i < 5; ++i) { const float a = lo16(xall[i]) - mean, bb = hi16(xall[i]) - mean; sq += a * a + bb * bb; } \
      const float var = wave_sum(sq) * (1.f / D); \
      const float mb = rbf(mean), sd = rbf(sqrtf(rbf(rbf(var) + 1e-5f))); \
      ln0 = rbf(rbf(lo16(xpair) - mb) / sd); ln1 = rbf(rbf(hi16(xpair) - mb) / sd); \
      asm volatile("" : "+v"(ln0), "+v"(ln1)); SB(); } while (0)
#define LDSAG() do { _Pragma("unroll") for (int i = 0; i < 4; ++i) sg[i] = ldsc4(rw, moff(tid < 320, WS_SAG + tid * 16), (i & 1) * 10240 + (i >> 1) * 5120); } while (0)
#define MAKEH() do { if (w < 5) { \
      if (__any((bad4(sg[0], tag) | bad4(sg[1], tag) | bad4(sg[2], tag) | bad4(sg[3], tag)) != 0u)) { \
        _Pragma("unroll 1") for (unsigned sp = 0; sp < SPIN_CAP; ++sp) { \
          MEMCLOB(); LDSAG(); \
          if (!__any((bad4(sg[0], tag) | bad4(sg[1], tag) | bad4(sg[2], tag) | bad4(sg[3], tag)) != 0u)) break; \
          __builtin_amdgcn_s_sleep(1); } } \
      const float sa0 = rbf(f32(sg[0].x) + f32(sg[1].x)), sa1 = rbf(f32(sg[0].z) + f32(sg[1].z)); \
      const float ca0 = rbf(f32(sg[2].x) + f32(sg[3].x)), ca1 = rbf(f32(sg[2].z) + f32(sg[3].z)); \
      ((LAS unsigned*)(L + LDS_VEC2))[tid] = pack2(rbf(ln0 * rbf(1.f + ca0)) + sa0, rbf(ln1 * rbf(1.f + ca1)) + sa1); } \
      lds_barrier(); } while (0)
    if (b < 40) {
      const int wi = 5 * xg + v, ga_ = wi < 20 ? wi : wi - 20, ha_ = wi < 20 ? 0 : 1, uu = b - 8;
      const bool two = v == 0 || (uu >= 12 && uu < 18);
      const int g0 = v == 0 ? FASTG(20 + xg) : (uu < 12 ? 8 * uu + 3 : FASTG(uu - 12));
      const int g1 = v == 0 ? FASTG(28 + xg) : FASTG(24 + uu);
      v4u ua[6];
      LDC();
      LDX();
      {
        const rsrc_t ra = mkrs(wmod + (size_t)(320 * ha_ + 40 * w) * (6 * D), 40 * 6 * D * 2);
#pragma unroll
        for (int i = 0; i < 6; ++i) ua[i] = ldnt(ra, 2 * r * (6 * D * 2) + ga_ * 128 + c * 16, (16 * (i >> 1) + (i & 1)) * (6 * D * 2));
        SB();
      }
      SILU();
      lds_barrier();
      unsigned hv[3];
      {
        const LAS unsigned* sc = (const LAS unsigned*)(L + LDS_VEC) + 160 * ha_ + 20 * w + r;
        hv[0] = sc[0]; hv[1] = sc[8]; hv[2] = sc[16]; hv[2] = r < 4 ? hv[2] : 0u;
      }
      unit_load<0, 5, true>(u0, wmod, wq, false, g0, w, lane);
      {
        unsigned pa[3][8];
#pragma unroll
        for (int i = 0; i < 3; ++i) pairperm(ua[2 * i], ua[2 * i + 1], pa[i]);
        float a[8];
#pragma unroll
        for (int k = 0; k < 8; ++k) a[k] = dot2(pa[2][k], hv[2], dot2(pa[1][k], hv[1], dot2(pa[0][k], hv[0], 0.f)));
        float y[4], z[2];
#pragma unroll
        for (int i = 0; i < 4; ++i) y[i] = fold32(a[i], a[i + 4]);
#pragma unroll
        for (int i = 0; i < 2; ++i) z[i] = fold16(y[i], y[i + 2]);
        const float t0 = z[0] + dpp<XOR8>(z[0]), t1 = z[1] + dpp<XOR8>(z[1]);
        ((LAS float*)(L + LDS_RED))[w * 64 + c * 8 + r] = (lane & 8) ? t1 : t0;
        lds_barrier();
        if (w == 0) {
          const LAS v2f* red = (const LAS v2f*)(L + LDS_RED) + (lane & 31);
          v2f sm = red[0];
#pragma unroll
          for (int i = 1; i < 8; ++i) sm += red[i * 32];
          stsc4(v4u{u32(sm.x), tag, u32(sm.y), tag}, rw, moff(lane < 32, WS_SAG + ha_ * 10240 + ga_ * 512 + lane * 16));
        }
      }
      SB();
      unit_load<5, 10, true>(u0, wmod, wq, false, g0, w, lane);
      LNX();
      if (two) unit_load<0, 10, true>(u1, wmod, wq, false, g1, w, lane);
      waitvm<2>();
      LDSAG();
      SB();
      unit_prep(u0, p0);
      if (two) unit_prep(u1, p1);
      SB();
      MAKEH();
      unit_partial(p0, L, LDS_VEC2, LDS_RED + 2048, w, lane);
      if (two) unit_partial(p1, L, LDS_VEC2, LDS_RED + 4096, w, lane);
      lds_barrier();
      if (w == 0 || (w == 1 && two)) unit_publish(L, LDS_RED + 2048 + w * 2048, lane, WS_QG + (w ? g1 : g0) * 256, tag, rw);
      SB();
      unit_load<0, 10, true>(u0, wmod, wq, true, 20 + b, w, lane);
      unit_prep(u0, p0);
      unit_partial(p0, L, LDS_VEC, LDS_RED, w, lane);
      lds_barrier();
      if (w == 0) unit_publish(L, LDS_RED, lane, WS_MODG + (20 + b) * 256, tag, rw);
    } else {
      const int f = 3 * px + pk - 5, fi = f - ((f + 4) >> 3);
      const int g0 = FASTG(42 + fi), g1 = FASTG(63 + fi);
      LDC();
      LDX();
      if ((f & 7) != 3) {
        unit_load<0, 6, false>(u0, wmod, wq, false, g0, w, lane);
        asm volatile("" : "+v"(tag), "+v"(xpair) : "v"(pf[0]), "v"(pf[1]), "v"(pf[2]), "v"(cpair));
        tag += 1u;
        unit_load<6, 10, true>(u0, wmod, wq, false, g0, w, lane);
        LNX();
        unit_load<0, 8, true>(u1, wmod, wq, false, g1, w, lane);
        unit_prep(u0, p0);
        unit_load<8, 10, true>(u1, wmod, wq, false, g1, w, lane);
        waitvm<2>();
        LDSAG();
        SB();
        unit_prep(u1, p1);
        SB();
        MAKEH();
        unit_partial(p0, L, LDS_VEC2, LDS_RED + 2048, w, lane);
        unit_partial(p1, L, LDS_VEC2, LDS_RED + 4096, w, lane);
        lds_barrier();
        if (w < 2) unit_publish(L, LDS_RED + 2048 + w * 2048, lane, WS_QG + (w ? g1 : g0) * 256, tag, rw);
        SB();
      } else {
        asm volatile("; no front work" : "+v"(tag), "+v"(xpair) : "v"(pf[0]), "v"(pf[1]), "v"(pf[2]), "v"(cpair));
        tag += 1u;
      }
      SB();
      const rsrc_t rd = mkrs(wd + (size_t)(f * 64 + 8 * w) * D, 8 * D * 2);
      const rsrc_t rg = mkrs(wg + f * 64, D * FF * 2 - f * 128), ru = mkrs(wu + f * 64, D * FF * 2 - f * 128);
      const int gvo = (40 * w + r) * (FF * 2) + c * 16;
      const int g4 = lane >> 4, q4 = (lane >> 2) & 3, p4 = lane & 3;
      v4u gu[20], dw[10];
#define LDD(nn, lo, hi) do { _Pragma("unroll") for (int i = lo; i < hi; ++i) { waitvm<CP>(); nn[i] = ldnt(rd, lane * 16, i * 1024); SB(); } } while (0)
#define LDGU(nn, p, lo, hi) do { _Pragma("unroll") for (int i = lo; i < hi; ++i) { waitvm<CP>(); nn[i] = ldnt(i < 5 ? rg : ru, gvo, (320 * (p) + 8 * (i % 5)) * FF * 2); SB(); } } while (0)
#define GUROUND(p, nn) do { \
        _Pragma("unroll") for (int i = 0; i < 5; ++i) { \
          const int row = 40 * w + 8 * i + r, a = row * 128 + ((c ^ ((row >> 1) & 7)) << 4); \
          *(LAS v4u*)(L + LDS_BIG + a) = nn[i]; *(LAS v4u*)(L + LDS_BIG + 40960 + a) = nn[5 + i]; } \
        lds_barrier(); \
        { const int row0 = 8 * g4 + q4, row1 = row0 + 4; \
          const LAS unsigned char* q0 = L + LDS_BIG + (p4 >> 1) * 40960 + row0 * 128 + ((w ^ ((row0 >> 1) & 7)) << 4) + (p4 & 1) * 8; \
          const LAS unsigned char* q1 = L + LDS_BIG + (p4 >> 1) * 40960 + row1 * 128 + ((w ^ ((row1 >> 1) & 7)) << 4) + (p4 & 1) * 8; \
          _Pragma("unroll") for (int ks = 0; ks < 10; ++ks) gu[(p) * 10 + ks] = trfrag(q0 + ks * 4096, q1 + ks * 4096); \
          PIN10(gu + (p) * 10); } \
        SB(); } while (0)
      LDGU(u0, 0, 0, 10);
      LDGU(u1, 1, 0, 6);
      GUROUND(0, u0);
      LDGU(u1, 1, 6, 10);
      LDD(u0, 0, 6);
      lds_barrier();
      GUROUND(1, u1);
      LDD(u0, 6, 10);
      lds_barrier();
#define STD(lo, hi) do { _Pragma("unroll") for (int i = lo; i < hi; ++i) { const int o_ = i * 1024 + lane * 16, rl_ = o_ / 1280; \
        *(LAS v4u*)(L + LDS_BIG + 8 * w * 1312 + o_ + rl_ * 32) = u0[i]; } SB(); } while (0)
      STD(0, 6);
      const bool red_on = (w == 5 || w == 6) && f < 20;
      const int opr = 16 * f + 8 * (w - 5) + c;
      const int pi = (tid & ~63) + 2 * rp + hf;
      v2u ma, mb2, mc;
      {
        const int o1 = WS_MODG + (w < 5 ? 640 + pi : 1600 + (red_on ? opr : 0)) * 8, o2 = w < 5 ? 2560 : 0;
#pragma unroll 1
        for (unsigned sp = 0; sp < SPIN_CAP; ++sp) {
          MEMCLOB();
          ma = ldsc2(rw, o1); mb2 = ldsc2(rw, o1 + o2); mc = ldsc2(rw, o1 + 2 * o2);
          if (!__any(((ma.y ^ tag) | (mb2.y ^ tag) | (mc.y ^ tag)) != 0u)) break;
          __builtin_amdgcn_s_sleep(4);
        }
        mc.x = pack2(1.f + lo16(mc.x), 1.f + hi16(mc.x));
        asm volatile("" : "+v"(mc.x));
      }
      STD(6, 10);
      lds_barrier();
      {
        const LAS unsigned char* q0 = L + LDS_BIG + (8 * g4 + q4) * 1312 + w * 160 + p4 * 8;
#pragma unroll
        for (int T = 0; T < 5; ++T)
#pragma unroll
          for (int ks = 0; ks < 2; ++ks) dw[2 * T + ks] = trfrag(q0 + ks * 32 * 1312 + T * 32, q0 + (ks * 32 + 4) * 1312 + T * 32);
        PIN10(dw);
        SB();
      }
      int dpo0 = WS_DP + (f * D + 80 * w + lane) * 8, dpo1 = moff(lane < 16, dpo0 + 512), dpr = WS_DP + r * (D * 8) + opr * 16;
      asm volatile("" : "+v"(dpo0), "+v"(dpo1), "+v"(dpr));
      LAS v2f* stat = (LAS v2f*)(L + LDS_QS + 1024);
      float x20 = 0.f, x21 = 0.f;
      xpair = (unsigned)__builtin_amdgcn_ds_bpermute((2 * rp + hf) * 4, (int)xpair);
      float xa = lo16(xpair), xb = hi16(xpair), ga0 = lo16(ma.x), ga1 = hi16(ma.x), sb0 = lo16(mb2.x), sb1 = hi16(mb2.x), cb0 = lo16(mc.x), cb1 = hi16(mc.x);
      int a_x2 = LDS_VEC2 + pi * 8, a_hb = LDS_HB + pi * 4, a_st = LDS_QS + 1024 + c * 8, a_h2 = LDS_QS + (8 * w + c) * 2, a_g4 = g4 * 16;
      asm volatile("" : "+v"(xa), "+v"(xb), "+v"(ga0), "+v"(ga1), "+v"(sb0), "+v"(sb1), "+v"(cb0), "+v"(cb1), "+v"(a_x2), "+v"(a_hb), "+v"(a_st), "+v"(a_h2), "+v"(a_g4));
      if (w >= 5) {
        stat[w] = v2f{0.f, 0.f};
      } else {
        v4u pv[8];
        const int wpoff = WS_WP + px * WP_REP + (hf * D + 128 * w + 4 * rp) * 4;
        const v4u t4 = tag4(tag);
        v4f sm;
#pragma unroll 1
        for (unsigned sp = 0; sp < SPIN_CAP; ++sp) {
          MEMCLOB();
#pragma unroll
          for (int g = 0; g < 8; ++g) pv[g] = ldsc4(rw, wpoff, (2 * g) * D * 4);
          unsigned bad = 0u;
#pragma unroll
          for (int g = 0; g < 8; ++g) {
            pv[g] ^= t4; bad |= pv[g].x | pv[g].y | pv[g].z | pv[g].w;
            { if (g == 0) sm = __builtin_bit_cast(v4f, pv[0]); else sm += __builtin_bit_cast(v4f, pv[g]); asm volatile("" : "+v"(sm), "+v"(bad)); }
          }
          if (!__any((bad & 0xffu) != 0u)) break;
        }
        const v2f at = {fold32(sm.x, sm.z), fold32(sm.y, sm.w)};
        x20 = rbf(xa + rbf(ga0 * rbf(at.x))); x21 = rbf(xb + rbf(ga1 * rbf(at.y)));
        *(LAS v2f*)(L + a_x2) = v2f{x20, x21};
        const float piv = __builtin_bit_cast(float, __builtin_amdgcn_readfirstlane(__builtin_bit_cast(int, x20)));
        const float d0 = x20 - piv, d1 = x21 - piv;
        const float s1 = wave_sum(d0 + d1), s2 = wave_sum(d0 * d0 + d1 * d1);
        stat[w] = v2f{piv + s1 * (1.f / 128), s2 - s1 * s1 * (1.f / 128)};
      }
      lds_barrier();
      if (w < 5) {
        const v2f st = *(const LAS v2f*)(L + a_st);
        const float mean = sum8(st.x) * 0.2f, dm = st.x - mean;
        const float m2 = sum8(c < 5 ? fmaf(128.f * dm, dm, st.y) : 0.f);
        const float mb = rbf(mean), sd = rbf(__builtin_amdgcn_sqrtf(rbf(rbf(fmaxf(m2, 0.f) * (1.f / D)) + 1e-5f)));
        const float rs = __builtin_amdgcn_rcpf(sd);
        const float l0 = rbf(rbf(x20 - mb) * rs), l1 = rbf(rbf(x21 - mb) * rs);
        *(LAS unsigned*)(L + a_hb) = pack2(rbf(l0 * cb0) + sb0, rbf(l1 * cb1) + sb1);
      }
      lds_barrier();
      {
        const LAS v4u* hb = (const LAS v4u*)(L + LDS_HB + a_g4);
        v4u ha[20];
#pragma unroll
        for (int ks = 0; ks < 20; ++ks) ha[ks] = hb[4 * ks];
        SB();
        v4f acc = {0.f, 0.f, 0.f, 0.f};
#pragma unroll
        for (int ks = 0; ks < 20; ++ks) acc = mfma16(ha[ks], gu[ks], acc);
        const float vv = rbf(acc.x);
        *(LAS __bf16*)(L + a_h2) = (__bf16)(vv * dpp<XOR8>(vv));
      }
      lds_barrier();
      {
        const LAS v4u* hp2 = (const LAS v4u*)(L + LDS_QS + a_g4);
        const v4u h0 = hp2[0], h1 = hp2[4];
        v4f acc[5];
#pragma unroll
        for (int T = 0; T < 5; ++T) acc[T] = mfma16(h0, dw[2 * T], v4f{0.f, 0.f, 0.f, 0.f});
#pragma unroll
        for (int T = 0; T < 5; ++T) acc[T] = mfma16(h1, dw[2 * T + 1], acc[T]);
        const float z = (lane & 32) ? ((lane & 16) ? acc[3].x : acc[2].x) : ((lane & 16) ? acc[1].x : acc[0].x);
        stsc2(v2u{u32(z), tag}, rw, dpo0);
        stsc2(v2u{u32(acc[4].x), tag}, rw, dpo1);
      }
      if (red_on) {
        const v2f x2v = ((const LAS v2f*)(L + LDS_VEC2))[opr];
        v4u dv[3];
#pragma unroll 1
        for (unsigned sp = 0; sp < SPIN_CAP; ++sp) {
          MEMCLOB();
#pragma unroll
          for (int i = 0; i < 3; ++i) dv[i] = __builtin_amdgcn_raw_buffer_load_b128(rw, dpr, i * 8 * D * 8, AUX_SC1);
          if (!__any((bad4(dv[0], tag) | bad4(dv[1], tag) | bad4(dv[2], tag)) != 0u)) break;
        }
        float z0 = (f32(dv[0].x) + f32(dv[1].x)) + f32(dv[2].x), z1 = (f32(dv[0].z) + f32(dv[1].z)) + f32(dv[2].z);
        z0 += dpp<XOR8>(z0); z1 += dpp<XOR8>(z1);
        z0 = fold16(z0, z0); z1 = fold16(z1, z1); z0 = fold32(z0, z0); z1 = fold32(z1, z1);
        const unsigned o = pack2(x2v.x + rbf(ga0 * rbf(z0)), x2v.y + rbf(ga1 * rbf(z1)));
        __builtin_amdgcn_raw_buffer_store_b32(o, mkrs(out, D * 2), moff(lane < 8, opr * 4), 0, 0);
        if (f == 0 && w == 5) stsc1(tag, rw, moff(lane == 0, WS_EP));
      }
    }
  }
}

extern "C" void kernel_launch(void* const* d_in, const int* in_sizes, int n_in, void* d_out, int out_size, void* d_ws, size_t ws_size,
                              hipStream_t stream) {
  hipLaunchKernelGGL(mega, dim3(256), dim3(512), 0, stream, (const u16*)d_in[0], (const u16*)d_in[1], (const u16*)d_in[2], (const u16*)d_in[3],
                     (const u16*)d_in[4], (const u16*)d_in[6], (const u16*)d_in[7], (const u16*)d_in[8], (const u16*)d_in[9], (const u16*)d_in[10],
                     (u16*)d_out, (unsigned char*)d_ws);
}
```
